```python
import torch

def _softmax(x, axis=-1):
    return torch.softmax(x, dim=axis)
def _gelu(x):
    return torch.nn.functional.gelu(x, approximate="tanh")
def _silu(x):
    return torch.nn.functional.silu(x)
def _sigmoid(x):
    return torch.sigmoid(x)
def _layernorm(x, axis=-1, eps=1e-5):
    m = x.mean(dim=axis, keepdim=True)
    v = x.var(dim=axis, keepdim=True, unbiased=False)
    return (x - m) / torch.sqrt(v + eps)
def _scatter_add(tgt, idx, src, axis=0):
    return tgt.clone().index_add_(axis, idx, src)
def _segment_sum(src, seg, n):
    out = torch.zeros((n,) + src.shape[1:], dtype=src.dtype)
    return out.index_add_(0, seg, src)
def _pad(x, pw):
    flat = tuple(v for pair in reversed(pw) for v in pair)
    return torch.nn.functional.pad(x, flat)

kernel_name = 'standalone__gqa_softmax__near_ridge'

def setup_inputs():

    _w = (lambda a: (lambda t: t.to(torch.bfloat16) if t.is_floating_point() else t)(torch.as_tensor(a)))
    B, Hq, Hkv, Sq, Sk, D = 8, 32, 32, 192, 1024, 128
    return {
        "q": _w((lambda *s: torch.randn(*s, dtype=torch.bfloat16) * 0.8)(B, Hq, Sq, D)),
        "k": _w((lambda *s: torch.randn(*s, dtype=torch.bfloat16) * 0.8)(B, Hkv, Sk, D)),
        "v": _w((lambda *s: torch.randn(*s, dtype=torch.bfloat16) * 0.05)(B, Hkv, Sk, D)),
    }

def reference(q, k, v):
    B, Hq, Sq, D = q.shape
    Hkv = k.shape[1]
    rep = Hq // Hkv
    k = (lambda x, n, axis: torch.repeat_interleave(x, n, dim=axis))(k, rep, axis=1)
    v = (lambda x, n, axis: torch.repeat_interleave(x, n, dim=axis))(v, rep, axis=1)
    scale = D ** -0.5
    scores = torch.einsum("bhqd,bhkd->bhqk", q, k) * scale
    p = _softmax(scores, axis=-1)
    return torch.einsum("bhqk,bhkd->bhqd", p, v)
```

```cpp
#include <hip/hip_runtime.h>
#include <hip/hip_bf16.h>
#include <stdint.h>

typedef __bf16   bf16x8 __attribute__((ext_vector_type(8)));
typedef __bf16   bf16x2 __attribute__((ext_vector_type(2)));
typedef short    s16x4  __attribute__((ext_vector_type(4)));
typedef float    f32x4  __attribute__((ext_vector_type(4)));
typedef float    f32x2  __attribute__((ext_vector_type(2)));
typedef unsigned u32x4  __attribute__((ext_vector_type(4)));
typedef unsigned u32x2  __attribute__((ext_vector_type(2)));
#define LAS __attribute__((address_space(3)))
#define DI __device__ __forceinline__

constexpr int NBH = 256, SQ = 192, SK = 1024, HD = 128;
constexpr int NT = SK / 64;
constexpr int NSLOT = 5;
constexpr int SLOTB = 32768;
constexpr int LDS_BYTES = NSLOT * SLOTB;
constexpr float SCL2 = 0.08838834764831845f * 1.4426950408889634f;
constexpr float THR = 8.0f;

DI unsigned cvtpk(float lo, float hi) { f32x2 v = {lo, hi}; bf16x2 b = __builtin_convertvector(v, bf16x2); return __builtin_bit_cast(unsigned, b); }
DI float xmax16(float x) { auto r = __builtin_amdgcn_permlane16_swap(__float_as_uint(x), __float_as_uint(x), false, false); return fmaxf(__uint_as_float(r[0]), __uint_as_float(r[1])); }
DI float xmax32(float x) { auto r = __builtin_amdgcn_permlane32_swap(__float_as_uint(x), __float_as_uint(x), false, false); return fmaxf(__uint_as_float(r[0]), __uint_as_float(r[1])); }
DI float xsum16(float x) { auto r = __builtin_amdgcn_permlane16_swap(__float_as_uint(x), __float_as_uint(x), false, false); return __uint_as_float(r[0]) + __uint_as_float(r[1]); }
DI float xsum32(float x) { auto r = __builtin_amdgcn_permlane32_swap(__float_as_uint(x), __float_as_uint(x), false, false); return __uint_as_float(r[0]) + __uint_as_float(r[1]); }
#define MFMA16(a, b, c) __builtin_amdgcn_mfma_f32_16x16x32_bf16((a), (b), (c), 0, 0, 0)
#define GLDS(m0v, voff, srd, soff) \
  asm volatile("s_mov_b32 m0, %0\n\ts_nop 0\n\tbuffer_load_dwordx4 %1, %2, %3 offen lds" :: "s"(m0v), "v"(voff), "s"(srd), "s"(soff) : "memory")

__global__ __launch_bounds__(512, 2)
void attn_fwd(const unsigned short* __restrict__ Qg, const unsigned short* __restrict__ Kg,
              const unsigned short* __restrict__ Vg, unsigned short* __restrict__ Og) {
  __shared__ __attribute__((aligned(1024))) char lds[LDS_BYTES];
  const unsigned tid = threadIdx.x, lane = tid & 63u;
  const unsigned wid = __builtin_amdgcn_readfirstlane(tid >> 6);
  const unsigned qg = wid & 3u, kh = wid >> 2;
  const unsigned r16 = lane & 15u, g = lane >> 4;
  const unsigned bh = blockIdx.x;
  const unsigned ldsb = (unsigned)(uintptr_t)(LAS char*)lds;

  const __amdgpu_buffer_rsrc_t srdQ = __builtin_amdgcn_make_buffer_rsrc((void*)(Qg + (size_t)bh * SQ * HD), (short)0, SQ * HD * 2, 0x00020000);
  const __amdgpu_buffer_rsrc_t srdK = __builtin_amdgcn_make_buffer_rsrc((void*)(Kg + (size_t)bh * SK * HD), (short)0, SK * HD * 2, 0x00020000);
  const __amdgpu_buffer_rsrc_t srdV = __builtin_amdgcn_make_buffer_rsrc((void*)(Vg + (size_t)bh * SK * HD), (short)0, SK * HD * 2, 0x00020000);
  const __amdgpu_buffer_rsrc_t srdO = __builtin_amdgcn_make_buffer_rsrc((void*)(Og + (size_t)bh * SQ * HD), (short)0, SQ * HD * 2, 0x00020000);

  u32x4 q[3][4];
  {
    const unsigned qv = (48u * qg + r16) * 256u + g * 16u;
#pragma unroll
    for (int qb = 0; qb < 3; ++qb) {
      const unsigned so = 4096u * qb;
      asm volatile("buffer_load_dwordx4 %0, %4, %5, %6 offen\n\t"
                   "buffer_load_dwordx4 %1, %4, %5, %6 offen offset:64\n\t"
                   "buffer_load_dwordx4 %2, %4, %5, %6 offen offset:128\n\t"
                   "buffer_load_dwordx4 %3, %4, %5, %6 offen offset:192"
                   : "=&v"(q[qb][0]), "=&v"(q[qb][1]), "=&v"(q[qb][2]), "=&v"(q[qb][3]) : "v"(qv), "s"(srdQ), "s"(so) : "memory");
    }
  }
  const unsigned prow = 4u * wid + g;
  const unsigned ksrc = prow * 256u + ((r16 ^ (prow & 15u)) << 4);
  const unsigned vsrc = prow * 256u + ((r16 ^ ((prow & 7u) << 1)) << 4);
  const unsigned mbase = ldsb + wid * 1024u;
  auto issue = [&](unsigned tile, unsigned slot) {
    const unsigned m = mbase + slot * SLOTB, so = tile * 16384u;
    GLDS(m, ksrc, srdK, so);
    GLDS(m + 8192u, ksrc, srdK, so + 8192u);
    GLDS(m + 16384u, vsrc, srdV, so);
    GLDS(m + 24576u, vsrc, srdV, so + 8192u);
  };
#pragma unroll
  for (int t = 0; t < NSLOT - 1; ++t) issue(t, t);

  unsigned kaddr[4], vaddr[8];
#pragma unroll
  for (int s = 0; s < 4; ++s) kaddr[s] = ldsb + (32u * kh + r16) * 256u + (((4u * s + g) ^ r16) << 4);
  {
    const unsigned qq = r16 >> 2, pp = r16 & 3u, rl = 4u * g + qq;
#pragma unroll
    for (int i = 0; i < 8; ++i) vaddr[i] = ldsb + 16384u + (32u * kh + rl) * 256u + ((((unsigned)i ^ (4u * kh) ^ (rl & 7u))) << 5) + pp * 8u;
  }
  asm volatile("s_waitcnt vmcnt(16)" : "+v"(q[0][0]), "+v"(q[0][1]), "+v"(q[0][2]), "+v"(q[0][3]), "+v"(q[1][0]), "+v"(q[1][1]), "+v"(q[1][2]), "+v"(q[1][3]),
               "+v"(q[2][0]), "+v"(q[2][1]), "+v"(q[2][2]), "+v"(q[2][3]) :: "memory");

  f32x4 o[8][3];
#pragma unroll
  for (int i = 0; i < 8; ++i)
#pragma unroll
    for (int qb = 0; qb < 3; ++qb) o[i][qb] = (f32x4){0.f, 0.f, 0.f, 0.f};
  float m[3] = {-INFINITY, -INFINITY, -INFINITY}, l[3] = {0.f, 0.f, 0.f};

  unsigned slot = 0, slot_prev = NSLOT - 1;
#pragma unroll 1
  for (int t = 0; t < NT; ++t) {
    asm volatile("s_waitcnt vmcnt(12) lgkmcnt(0)\n\ts_barrier" ::: "memory");
    issue(t + NSLOT - 1, slot_prev);
    const unsigned sb = slot * SLOTB;
    f32x4 sc[2][3];
#pragma unroll
    for (int kb = 0; kb < 2; ++kb)
#pragma unroll
      for (int qb = 0; qb < 3; ++qb) sc[kb][qb] = (f32x4){0.f, 0.f, 0.f, 0.f};
#pragma unroll
    for (int s = 0; s < 4; ++s)
#pragma unroll
      for (int kb = 0; kb < 2; ++kb) {
        const bf16x8 kf = *(const LAS bf16x8*)(uintptr_t)(kaddr[s] + sb + 4096u * kb);
#pragma unroll
        for (int qb = 0; qb < 3; ++qb) sc[kb][qb] = MFMA16(kf, __builtin_bit_cast(bf16x8, q[qb][s]), sc[kb][qb]);
      }
    float mx[3];
    bool grow = false;
#pragma unroll
    for (int qb = 0; qb < 3; ++qb) {
      float a = fmaxf(fmaxf(sc[0][qb][0], sc[0][qb][1]), sc[0][qb][2]);
      a = fmaxf(fmaxf(a, sc[0][qb][3]), sc[1][qb][0]);
      a = fmaxf(fmaxf(a, sc[1][qb][1]), sc[1][qb][2]);
      a = fmaxf(a, sc[1][qb][3]);
      mx[qb] = a * SCL2;
      grow |= (mx[qb] > m[qb] + THR);
    }
    if (__builtin_expect(__any((int)grow), 0)) {
#pragma unroll
      for (int qb = 0; qb < 3; ++qb) {
        const float rm = xmax32(xmax16(mx[qb]));
        const float mn = fmaxf(m[qb], rm);
        const float al = __builtin_amdgcn_exp2f(m[qb] - mn);
        m[qb] = mn; l[qb] *= al;
#pragma unroll
        for (int i = 0; i < 8; ++i) o[i][qb] *= al;
      }
    }
    bf16x8 p[3];
#pragma unroll
    for (int qb = 0; qb < 3; ++qb) {
      float e[8];
#pragma unroll
      for (int j = 0; j < 8; ++j) e[j] = __builtin_amdgcn_exp2f(__builtin_fmaf(sc[j >> 2][qb][j & 3], SCL2, -m[qb]));
      l[qb] += ((e[0] + e[1]) + (e[2] + e[3])) + ((e[4] + e[5]) + (e[6] + e[7]));
      u32x4 w = {cvtpk(e[0], e[1]), cvtpk(e[2], e[3]), cvtpk(e[4], e[5]), cvtpk(e[6], e[7])};
      p[qb] = __builtin_bit_cast(bf16x8, w);
    }
#pragma unroll
    for (int i = 0; i < 8; ++i) {
      const s16x4 lo = __builtin_amdgcn_ds_read_tr16_b64_v4i16((LAS s16x4*)(uintptr_t)(vaddr[i] + sb));
      const s16x4 hi = __builtin_amdgcn_ds_read_tr16_b64_v4i16((LAS s16x4*)(uintptr_t)(vaddr[i] + sb + 4096u));
      const bf16x8 vf = __builtin_bit_cast(bf16x8, __builtin_shufflevector(lo, hi, 0, 1, 2, 3, 4, 5, 6, 7));
#pragma unroll
      for (int qb = 0; qb < 3; ++qb) o[i][qb] = MFMA16(vf, p[qb], o[i][qb]);
    }
    slot_prev = slot; slot = (slot == NSLOT - 1) ? 0u : slot + 1u;
  }

  asm volatile("s_waitcnt vmcnt(0) lgkmcnt(0)\n\ts_barrier" ::: "memory");
#pragma unroll
  for (int qb = 0; qb < 3; ++qb) l[qb] = xsum32(xsum16(l[qb]));
  {
    LAS char* xb = (LAS char*)lds + wid * 16384u + lane * 16u;
#pragma unroll
    for (int i = 0; i < 4; ++i)
#pragma unroll
      for (int qb = 0; qb < 3; ++qb) *(LAS f32x4*)(xb + (i * 3 + qb) * 1024) = o[4 + i][qb];
    *(LAS f32x4*)(xb + 12 * 1024) = (f32x4){m[0], m[1], m[2], 0.f};
    *(LAS f32x4*)(xb + 13 * 1024) = (f32x4){l[0], l[1], l[2], 0.f};
  }
  asm volatile("s_waitcnt lgkmcnt(0)\n\ts_barrier" ::: "memory");
  {
    const LAS char* xb = (const LAS char*)lds + (wid ^ 4u) * 16384u + lane * 16u;
    const f32x4 mo = *(const LAS f32x4*)(xb + 12 * 1024), lo = *(const LAS f32x4*)(xb + 13 * 1024);
    const unsigned ov = (48u * qg + r16) * 256u + (kh * 64u + (g & 1u) * 16u + (g >> 1) * 8u) * 2u;
#pragma unroll
    for (int qb = 0; qb < 3; ++qb) {
      const float mn = fmaxf(m[qb], mo[qb]);
      const float fa = __builtin_amdgcn_exp2f(m[qb] - mn), fb = __builtin_amdgcn_exp2f(mo[qb] - mn);
      const float inv = 1.0f / (l[qb] * fa + lo[qb] * fb);
      const float sa = fa * inv, sb2 = fb * inv;
      u32x2 w[4];
#pragma unroll
      for (int i = 0; i < 4; ++i) {
        const f32x4 x = *(const LAS f32x4*)(xb + (i * 3 + qb) * 1024);
        const f32x4 y = o[i][qb] * sa + x * sb2;
        w[i] = (u32x2){cvtpk(y[0], y[1]), cvtpk(y[2], y[3])};
      }
#pragma unroll
      for (int j = 0; j < 2; ++j) {
        auto r0 = __builtin_amdgcn_permlane16_swap(w[2 * j][0], w[2 * j + 1][0], false, false);
        auto r1 = __builtin_amdgcn_permlane16_swap(w[2 * j][1], w[2 * j + 1][1], false, false);
        const u32x4 out = {r0[0], r1[0], r0[1], r1[1]};
        __builtin_amdgcn_raw_buffer_store_b128(out, srdO, ov + 4096u * qb + 64u * j, 0, 0);
      }
    }
  }
}

extern "C" void kernel_launch(void* const* d_in, const int* in_sizes, int n_in, void* d_out, int out_size,
                              void* d_ws, size_t ws_size, hipStream_t stream) {
  (void)in_sizes; (void)n_in; (void)out_size; (void)d_ws; (void)ws_size;
  hipLaunchKernelGGL(attn_fwd, dim3(NBH), dim3(512), 0, stream, (const unsigned short*)d_in[0], (const unsigned short*)d_in[1],
                     (const unsigned short*)d_in[2], (unsigned short*)d_out);
}
```

```cpp
#include <hip/hip_runtime.h>
#include <hip/hip_bf16.h>
#include <stdint.h>

typedef unsigned u32x4 __attribute__((ext_vector_type(4)));
#define LAS __attribute__((address_space(3)))

constexpr int NBH = 256, SQ = 192, SK = 1024, HD = 128;
constexpr unsigned KRING = 0, VRING = 65536, QSTG = 114688, LDS_BYTES = 163840;

#define GLDS(aux, m0v, voff, srd, soff) \
  asm volatile("s_mov_b32 m0, %0\n\ts_nop 0\n\tbuffer_load_dwordx4 %1, %2, %3 offen " aux " lds" :: "s"(m0v), "v"(voff), "s"(srd), "s"(soff) : "memory")

__global__ __launch_bounds__(256, 1)
void attn_fwd(const unsigned short* __restrict__ Qg, const unsigned short* __restrict__ Kg,
              const unsigned short* __restrict__ Vg, unsigned short* __restrict__ Og) {
  __shared__ __attribute__((aligned(1024))) char lds[LDS_BYTES];
  const unsigned tid = threadIdx.x, lane = tid & 63u;
  const unsigned wid = __builtin_amdgcn_readfirstlane(tid >> 6);
  const unsigned r16 = lane & 15u, g = lane >> 4;
  const unsigned bh = (blockIdx.x & 7u) * 32u + (blockIdx.x >> 3);
  const unsigned ldsb = (unsigned)(uintptr_t)(LAS char*)lds;

  const __amdgpu_buffer_rsrc_t srdQ = __builtin_amdgcn_make_buffer_rsrc((void*)(Qg + (size_t)bh * SQ * HD), (short)0, SQ * HD * 2, 0x00020000);
  const __amdgpu_buffer_rsrc_t srdK = __builtin_amdgcn_make_buffer_rsrc((void*)(Kg + (size_t)bh * SK * HD), (short)0, SK * HD * 2, 0x00020000);
  const __amdgpu_buffer_rsrc_t srdV = __builtin_amdgcn_make_buffer_rsrc((void*)(Vg + (size_t)bh * SK * HD), (short)0, SK * HD * 2, 0x00020000);
  const __amdgpu_buffer_rsrc_t srdO = __builtin_amdgcn_make_buffer_rsrc((void*)(Og + (size_t)bh * SQ * HD), (short)0, SQ * HD * 2, 0x00020000);

  const unsigned prow = 8u * wid + (lane >> 3);
  const unsigned ksrc = prow * 256u + (((lane & 7u) ^ ((prow >> 1) & 7u)) << 4);
  const unsigned vsrc = prow * 256u + (((lane & 7u) ^ (((prow >> 1) & 3u) << 1)) << 4);
  const unsigned mb = ldsb + wid * 1024u;
  unsigned xcc; asm volatile("s_getreg_b32 %0, hwreg(HW_REG_XCC_ID, 0, 4)" : "=s"(xcc));
  {
    unsigned long long pc; asm volatile("s_getpc_b64 %0" : "=s"(pc));
    const __amdgpu_buffer_rsrc_t srdC = __builtin_amdgcn_make_buffer_rsrc((void*)pc, (short)0, 9280, 0x00020000);
    asm volatile("s_mov_b32 m0, %0\n\ts_nop 0\n\tbuffer_load_dword %1, %2, 0 offen lds" :: "s"(mb + VRING + 32768u), "v"(tid * 64u), "s"(srdC) : "memory");
  }
  GLDS("nt", mb + KRING + 49152u + 4096u, ksrc, srdK, 0u); GLDS("nt", mb + KRING + 49152u + 12288u, ksrc, srdK, 128u);
  {
    const unsigned q0 = (lane >> 3) * 256u + (((lane & 7u) ^ (lane >> 4)) << 4), q1 = q0 ^ 64u;
    const unsigned m = ldsb + QSTG + wid * 12288u, so = wid * 12288u;
#pragma unroll
    for (unsigned i = 0; i < 12; ++i) {
      const unsigned rg = 2u * (i >> 2) + (i & 1u), dh = (i >> 1) & 1u;
      GLDS("nt", m + dh * 6144u + rg * 1024u, (rg & 1u) ? q1 : q0, srdQ, so + rg * 2048u + dh * 128u);
    }
  }
  const unsigned kl0 = r16 * 128u + (((0u + g) ^ ((r16 >> 1) & 7u)) << 4), kl1 = r16 * 128u + (((4u + g) ^ ((r16 >> 1) & 7u)) << 4);
  u32x4 ka = {ldsb + KRING + kl0, ldsb + KRING + kl1, ksrc, vsrc}, va;
  {
    const unsigned rl = 4u * g + (r16 >> 2), pp = r16 & 3u;
#pragma unroll
    for (int j = 0; j < 4; ++j) va[j] = ldsb + VRING + rl * 128u + (((unsigned)j ^ ((rl >> 1) & 3u)) << 5) + pp * 8u;
  }
  asm volatile(
    "s_mov_b32 s68, 0x3e0293ee\n\t"
    "s_mov_b32 s69, 0xffff0000\n\t"
    "s_mov_b32 s75, 16384\n\t"
    "s_mov_b32 s62, 0\n\t"
    "s_mov_b32 s74, %[mb]\n\t"
    "s_add_u32 s63, %[mb], 16384\n\t"
    "s_mov_b32 s67, 0\n\t"
    "s_mov_b32 s76, 0\n\t"
    "s_mov_b32 s65, 0xbf800000\n\t"
    "s_mov_b32 s60, 0x44800000\n\t"
    "s_mov_b32 s61, 0\n\t"
    "s_mov_b64 s[84:85], %[seq]\n\t"
    "s_mov_b64 s[86:87], %[dl]\n\t"
    "s_mov_b32 s89, 1\n\t"
    "s_mov_b32 s77, %[p2]\n\t"
    "s_mov_b32 s92, %[pss]\n\t"
    "v_mov_b64 v[0:1], 0\n\t"
    "v_mov_b64 v[2:3], 0\n\t"
    "v_mov_b64 v[4:5], 0\n\t"
    "v_mov_b64 v[6:7], 0\n\t"
    "v_mov_b64 v[8:9], 0\n\t"
    "v_mov_b64 v[10:11], 0\n\t"
    "v_mov_b64 v[12:13], 0\n\t"
    "v_mov_b64 v[14:15], 0\n\t"
    "v_mov_b64 v[16:17], 0\n\t"
    "v_mov_b64 v[18:19], 0\n\t"
    "v_mov_b64 v[20:21], 0\n\t"
    "v_mov_b64 v[22:23], 0\n\t"
    "v_mov_b64 v[24:25], 0\n\t"
    "v_mov_b64 v[26:27], 0\n\t"
    "v_mov_b64 v[28:29], 0\n\t"
    "v_mov_b64 v[30:31], 0\n\t"
    "v_mov_b64 v[32:33], 0\n\t"
    "v_mov_b64 v[34:35], 0\n\t"
    "v_mov_b64 v[36:37], 0\n\t"
    "v_mov_b64 v[38:39], 0\n\t"
    "v_mov_b64 v[40:41], 0\n\t"
    "v_mov_b64 v[42:43], 0\n\t"
    "v_mov_b64 v[44:45], 0\n\t"
    "v_mov_b64 v[46:47], 0\n\t"
    "v_mov_b64 v[48:49], 0\n\t"
    "v_mov_b64 v[50:51], 0\n\t"
    "v_mov_b64 v[52:53], 0\n\t"
    "v_mov_b64 v[54:55], 0\n\t"
    "v_mov_b64 v[56:57], 0\n\t"
    "v_mov_b64 v[58:59], 0\n\t"
    "v_mov_b64 v[60:61], 0\n\t"
    "v_mov_b64 v[62:63], 0\n\t"
    "v_mov_b64 v[64:65], 0\n\t"
    "v_mov_b64 v[66:67], 0\n\t"
    "v_mov_b64 v[68:69], 0\n\t"
    "v_mov_b64 v[70:71], 0\n\t"
    "v_mov_b64 v[72:73], 0\n\t"
    "v_mov_b64 v[74:75], 0\n\t"
    "v_mov_b64 v[76:77], 0\n\t"
    "v_mov_b64 v[78:79], 0\n\t"
    "v_mov_b64 v[80:81], 0\n\t"
    "v_mov_b64 v[82:83], 0\n\t"
    "v_mov_b64 v[84:85], 0\n\t"
    "v_mov_b64 v[86:87], 0\n\t"
    "v_mov_b64 v[88:89], 0\n\t"
    "v_mov_b64 v[90:91], 0\n\t"
    "v_mov_b64 v[92:93], 0\n\t"
    "v_mov_b64 v[94:95], 0\n\t"
    "v_mov_b64 v[204:205], 0\n\t"
    "v_mov_b64 v[206:207], 0\n\t"
    "v_mov_b64 v[208:209], 0\n\t"
    "v_mov_b64 v[210:211], 0\n\t"
    "v_mov_b64 v[212:213], 0\n\t"
    "v_mov_b64 v[214:215], 0\n\t"
    "v_mov_b64 v[216:217], 0\n\t"
    "v_mov_b64 v[218:219], 0\n\t"
    "v_mov_b64 v[220:221], 0\n\t"
    "v_mov_b64 v[222:223], 0\n\t"
    "v_mov_b64 v[224:225], 0\n\t"
    "v_mov_b64 v[226:227], 0\n\t"
    "v_mov_b64 v[236:237], 0\n\t"
    "v_mov_b64 v[238:239], 0\n\t"
    "v_accvgpr_write_b32 a32, 0\n\t"
    "v_accvgpr_write_b32 a33, 0\n\t"
    "v_accvgpr_write_b32 a34, 0\n\t"
    "v_accvgpr_write_b32 a35, 0\n\t"
    "v_accvgpr_write_b32 a36, 0\n\t"
    "v_accvgpr_write_b32 a37, 0\n\t"
    "v_accvgpr_write_b32 a38, 0\n\t"
    "v_accvgpr_write_b32 a39, 0\n\t"
    "v_accvgpr_write_b32 a40, 0\n\t"
    "v_accvgpr_write_b32 a41, 0\n\t"
    "v_accvgpr_write_b32 a42, 0\n\t"
    "v_accvgpr_write_b32 a43, 0\n\t"
    "v_accvgpr_write_b32 a44, 0\n\t"
    "v_accvgpr_write_b32 a45, 0\n\t"
    "v_accvgpr_write_b32 a46, 0\n\t"
    "v_accvgpr_write_b32 a47, 0\n\t"
    "v_accvgpr_write_b32 a48, 0\n\t"
    "v_accvgpr_write_b32 a49, 0\n\t"
    "v_accvgpr_write_b32 a50, 0\n\t"
    "v_accvgpr_write_b32 a51, 0\n\t"
    "v_accvgpr_write_b32 a52, 0\n\t"
    "v_accvgpr_write_b32 a53, 0\n\t"
    "v_accvgpr_write_b32 a54, 0\n\t"
    "v_accvgpr_write_b32 a55, 0\n\t"
    "v_accvgpr_write_b32 a56, 0\n\t"
    "v_accvgpr_write_b32 a57, 0\n\t"
    "v_accvgpr_write_b32 a58, 0\n\t"
    "v_accvgpr_write_b32 a59, 0\n\t"
    "v_accvgpr_write_b32 a60, 0\n\t"
    "v_accvgpr_write_b32 a61, 0\n\t"
    "v_accvgpr_write_b32 a62, 0\n\t"
    "v_accvgpr_write_b32 a63, 0\n\t"
    "v_mov_b32 v252, 0x3f803f80\n\t"
    "v_accvgpr_write_b32 a64, v252\n\t"
    "v_accvgpr_write_b32 a65, v252\n\t"
    "v_accvgpr_write_b32 a66, v252\n\t"
    "v_accvgpr_write_b32 a67, v252\n\t"
    "v_add_u32 v240, %[qo], v244\n\t"
    "v_add_u32 v241, %[qo], v245\n\t"
    "s_waitcnt vmcnt(12)\n\t"
    "s_barrier\n\t"
    "s_memrealtime s[80:81]\n\t"
    "s_waitcnt lgkmcnt(0)\n\t"
    "s_mov_b32 s88, s80\n\t"
    "ds_read_b128 a[0:3], v244 offset:53248\n\t"
    "ds_read_b128 a[4:7], v244 offset:55296\n\t"
    "ds_read_b128 a[8:11], v245 offset:53248\n\t"
    "ds_read_b128 a[12:15], v245 offset:55296\n\t"
    "ds_read_b128 a[16:19], v244 offset:61440\n\t"
    "ds_read_b128 a[20:23], v244 offset:63488\n\t"
    "ds_read_b128 a[24:27], v245 offset:61440\n\t"
    "ds_read_b128 a[28:31], v245 offset:63488\n\t"
    "s_add_u32 m0, s74, 65536\n\t"
    "s_nop 0\n\t"
    "buffer_load_dwordx4 v247, %[srdv], s62 offen nt lds\n\t"
    "s_add_u32 m0, s74, 73600\n\t"
    "s_nop 0\n\t"
    "buffer_load_dwordx4 v247, %[srdv], s62 offen offset:128 nt lds\n\t"
    "s_add_u32 s62, s62, 8192\n\t"
    "s_waitcnt vmcnt(10)\n\t"
    "ds_read_b128 v[96:99], v240 offset:0\n\t"
    "ds_read_b128 v[100:103], v241 offset:0\n\t"
    "ds_read_b128 v[104:107], v240 offset:6144\n\t"
    "ds_read_b128 v[108:111], v241 offset:6144\n\t"
    "s_add_u32 m0, s74, 0\n\t"
    "s_nop 0\n\t"
    "buffer_load_dwordx4 v246, %[srdk], s62 offen nt lds\n\t"
    "s_add_u32 m0, s74, 8064\n\t"
    "s_nop 0\n\t"
    "buffer_load_dwordx4 v246, %[srdk], s62 offen offset:128 nt lds\n\t"
    "s_waitcnt lgkmcnt(3)\n\t"
    "v_lshlrev_b32 v228, 16, v96\n\t"
    "v_and_b32 v229, s69, v96\n\t"
    "v_lshlrev_b32 v230, 16, v97\n\t"
    "v_and_b32 v231, s69, v97\n\t"
    "v_pk_mul_f32 v[228:229], v[228:229], s[68:69] op_sel_hi:[1,0]\n\t"
    "v_pk_mul_f32 v[230:231], v[230:231], s[68:69] op_sel_hi:[1,0]\n\t"
    "v_cvt_pk_bf16_f32 v96, v228, v229\n\t"
    "v_cvt_pk_bf16_f32 v97, v230, v231\n\t"
    "v_lshlrev_b32 v232, 16, v98\n\t"
    "v_and_b32 v233, s69, v98\n\t"
    "v_lshlrev_b32 v234, 16, v99\n\t"
    "v_and_b32 v235, s69, v99\n\t"
    "v_pk_mul_f32 v[232:233], v[232:233], s[68:69] op_sel_hi:[1,0]\n\t"
    "v_pk_mul_f32 v[234:235], v[234:235], s[68:69] op_sel_hi:[1,0]\n\t"
    "v_cvt_pk_bf16_f32 v98, v232, v233\n\t"
    "v_cvt_pk_bf16_f32 v99, v234, v235\n\t"
    "s_waitcnt lgkmcnt(2)\n\t"
    "v_lshlrev_b32 v228, 16, v100\n\t"
    "v_and_b32 v229, s69, v100\n\t"
    "v_lshlrev_b32 v230, 16, v101\n\t"
    "v_and_b32 v231, s69, v101\n\t"
    "v_pk_mul_f32 v[228:229], v[228:229], s[68:69] op_sel_hi:[1,0]\n\t"
    "v_pk_mul_f32 v[230:231], v[230:231], s[68:69] op_sel_hi:[1,0]\n\t"
    "v_cvt_pk_bf16_f32 v100, v228, v229\n\t"
    "v_cvt_pk_bf16_f32 v101, v230, v231\n\t"
    "v_lshlrev_b32 v232, 16, v102\n\t"
    "v_and_b32 v233, s69, v102\n\t"
    "v_lshlrev_b32 v234, 16, v103\n\t"
    "v_and_b32 v235, s69, v103\n\t"
    "v_pk_mul_f32 v[232:233], v[232:233], s[68:69] op_sel_hi:[1,0]\n\t"
    "v_pk_mul_f32 v[234:235], v[234:235], s[68:69] op_sel_hi:[1,0]\n\t"
    "v_cvt_pk_bf16_f32 v102, v232, v233\n\t"
    "v_cvt_pk_bf16_f32 v103, v234, v235\n\t"
    "v_mfma_f32_16x16x32_bf16 v[144:147], a[0:3], v[96:99], 0\n\t"
    "v_mfma_f32_16x16x32_bf16 v[156:159], a[4:7], v[96:99], 0\n\t"
    "s_waitcnt lgkmcnt(1)\n\t"
    "v_lshlrev_b32 v228, 16, v104\n\t"
    "v_and_b32 v229, s69, v104\n\t"
    "v_lshlrev_b32 v230, 16, v105\n\t"
    "v_and_b32 v231, s69, v105\n\t"
    "v_pk_mul_f32 v[228:229], v[228:229], s[68:69] op_sel_hi:[1,0]\n\t"
    "v_pk_mul_f32 v[230:231], v[230:231], s[68:69] op_sel_hi:[1,0]\n\t"
    "v_cvt_pk_bf16_f32 v104, v228, v229\n\t"
    "v_cvt_pk_bf16_f32 v105, v230, v231\n\t"
    "v_lshlrev_b32 v232, 16, v106\n\t"
    "v_and_b32 v233, s69, v106\n\t"
    "v_lshlrev_b32 v234, 16, v107\n\t"
    "v_and_b32 v235, s69, v107\n\t"
    "v_pk_mul_f32 v[232:233], v[232:233], s[68:69] op_sel_hi:[1,0]\n\t"
    "v_pk_mul_f32 v[234:235], v[234:235], s[68:69] op_sel_hi:[1,0]\n\t"
    "v_cvt_pk_bf16_f32 v106, v232, v233\n\t"
    "v_cvt_pk_bf16_f32 v107, v234, v235\n\t"
    "v_mfma_f32_16x16x32_bf16 v[144:147], a[8:11], v[100:103], v[144:147]\n\t"
    "v_mfma_f32_16x16x32_bf16 v[156:159], a[12:15], v[100:103], v[156:159]\n\t"
    "s_waitcnt lgkmcnt(0)\n\t"
    "v_lshlrev_b32 v228, 16, v108\n\t"
    "v_and_b32 v229, s69, v108\n\t"
    "v_lshlrev_b32 v230, 16, v109\n\t"
    "v_and_b32 v231, s69, v109\n\t"
    "v_pk_mul_f32 v[228:229], v[228:229], s[68:69] op_sel_hi:[1,0]\n\t"
    "v_pk_mul_f32 v[230:231], v[230:231], s[68:69] op_sel_hi:[1,0]\n\t"
    "v_cvt_pk_bf16_f32 v108, v228, v229\n\t"
    "v_cvt_pk_bf16_f32 v109, v230, v231\n\t"
    "v_lshlrev_b32 v232, 16, v110\n\t"
    "v_and_b32 v233, s69, v110\n\t"
    "v_lshlrev_b32 v234, 16, v111\n\t"
    "v_and_b32 v235, s69, v111\n\t"
    "v_pk_mul_f32 v[232:233], v[232:233], s[68:69] op_sel_hi:[1,0]\n\t"
    "v_pk_mul_f32 v[234:235], v[234:235], s[68:69] op_sel_hi:[1,0]\n\t"
    "v_cvt_pk_bf16_f32 v110, v232, v233\n\t"
    "v_cvt_pk_bf16_f32 v111, v234, v235\n\t"
    "v_mfma_f32_16x16x32_bf16 v[144:147], a[16:19], v[104:107], v[144:147]\n\t"
    "v_mfma_f32_16x16x32_bf16 v[156:159], a[20:23], v[104:107], v[156:159]\n\t"
    "s_nop 1\n\t"
    "v_mfma_f32_16x16x32_bf16 v[144:147], a[24:27], v[108:111], v[144:147]\n\t"
    "v_mfma_f32_16x16x32_bf16 v[156:159], a[28:31], v[108:111], v[156:159]\n\t"
    "s_nop 7\n\t"
    "v_max3_f32 v242, v144, v145, v146\n\t"
    "v_max3_f32 v243, v147, v156, v157\n\t"
    "v_max3_f32 v242, v242, v158, v159\n\t"
    "v_max_f32 v242, v242, v243\n\t"
    "v_mov_b32 v243, v242\n\t"
    "s_nop 1\n\t"
    "v_permlane16_swap_b32 v242, v243\n\t"
    "s_nop 1\n\t"
    "v_max_f32 v242, v242, v243\n\t"
    "v_mov_b32 v243, v242\n\t"
    "s_nop 1\n\t"
    "v_permlane32_swap_b32 v242, v243\n\t"
    "s_nop 1\n\t"
    "v_max_f32 v242, v242, v243\n\t"
    "v_sub_f32 v192, 0, v242\n\t"
    "v_sub_f32 v193, 0, v242\n\t"
    "v_sub_f32 v194, 0, v242\n\t"
    "v_sub_f32 v195, 0, v242\n\t"
    "v_sub_f32 v144, v144, v242\n\t"
    "v_sub_f32 v145, v145, v242\n\t"
    "v_sub_f32 v146, v146, v242\n\t"
    "v_sub_f32 v147, v147, v242\n\t"
    "v_sub_f32 v156, v156, v242\n\t"
    "v_sub_f32 v157, v157, v242\n\t"
    "v_sub_f32 v158, v158, v242\n\t"
    "v_sub_f32 v159, v159, v242\n\t"
    "s_waitcnt vmcnt(8)\n\t"
    "ds_read_b128 v[112:115], v240 offset:2048\n\t"
    "ds_read_b128 v[116:119], v241 offset:2048\n\t"
    "ds_read_b128 v[120:123], v240 offset:8192\n\t"
    "ds_read_b128 v[124:127], v241 offset:8192\n\t"
    "s_add_u32 m0, s74, 69632\n\t"
    "s_nop 0\n\t"
    "buffer_load_dwordx4 v247, %[srdv], s62 offen nt lds\n\t"
    "s_add_u32 m0, s74, 77696\n\t"
    "s_nop 0\n\t"
    "buffer_load_dwordx4 v247, %[srdv], s62 offen offset:128 nt lds\n\t"
    "s_add_u32 s62, s62, 8192\n\t"
    "s_add_u32 m0, s74, 4096\n\t"
    "s_nop 0\n\t"
    "buffer_load_dwordx4 v246, %[srdk], s62 offen nt lds\n\t"
    "s_add_u32 m0, s74, 12160\n\t"
    "s_nop 0\n\t"
    "buffer_load_dwordx4 v246, %[srdk], s62 offen offset:128 nt lds\n\t"
    "s_and_b32 s74, s62, 0xc000\n\t"
    "s_add_u32 s74, s74, %[mb]\n\t"
    "s_waitcnt lgkmcnt(3)\n\t"
    "v_lshlrev_b32 v228, 16, v112\n\t"
    "v_and_b32 v229, s69, v112\n\t"
    "v_lshlrev_b32 v230, 16, v113\n\t"
    "v_and_b32 v231, s69, v113\n\t"
    "v_pk_mul_f32 v[228:229], v[228:229], s[68:69] op_sel_hi:[1,0]\n\t"
    "v_pk_mul_f32 v[230:231], v[230:231], s[68:69] op_sel_hi:[1,0]\n\t"
    "v_cvt_pk_bf16_f32 v112, v228, v229\n\t"
    "v_cvt_pk_bf16_f32 v113, v230, v231\n\t"
    "v_lshlrev_b32 v232, 16, v114\n\t"
    "v_and_b32 v233, s69, v114\n\t"
    "v_lshlrev_b32 v234, 16, v115\n\t"
    "v_and_b32 v235, s69, v115\n\t"
    "v_pk_mul_f32 v[232:233], v[232:233], s[68:69] op_sel_hi:[1,0]\n\t"
    "v_pk_mul_f32 v[234:235], v[234:235], s[68:69] op_sel_hi:[1,0]\n\t"
    "v_cvt_pk_bf16_f32 v114, v232, v233\n\t"
    "v_cvt_pk_bf16_f32 v115, v234, v235\n\t"
    "s_waitcnt lgkmcnt(2)\n\t"
    "v_lshlrev_b32 v228, 16, v116\n\t"
    "v_and_b32 v229, s69, v116\n\t"
    "v_lshlrev_b32 v230, 16, v117\n\t"
    "v_and_b32 v231, s69, v117\n\t"
    "v_pk_mul_f32 v[228:229], v[228:229], s[68:69] op_sel_hi:[1,0]\n\t"
    "v_pk_mul_f32 v[230:231], v[230:231], s[68:69] op_sel_hi:[1,0]\n\t"
    "v_cvt_pk_bf16_f32 v116, v228, v229\n\t"
    "v_cvt_pk_bf16_f32 v117, v230, v231\n\t"
    "v_lshlrev_b32 v232, 16, v118\n\t"
    "v_and_b32 v233, s69, v118\n\t"
    "v_lshlrev_b32 v234, 16, v119\n\t"
    "v_and_b32 v235, s69, v119\n\t"
    "v_pk_mul_f32 v[232:233], v[232:233], s[68:69] op_sel_hi:[1,0]\n\t"
    "v_pk_mul_f32 v[234:235], v[234:235], s[68:69] op_sel_hi:[1,0]\n\t"
    "v_cvt_pk_bf16_f32 v118, v232, v233\n\t"
    "v_cvt_pk_bf16_f32 v119, v234, v235\n\t"
    "v_mfma_f32_16x16x32_bf16 v[148:151], a[0:3], v[112:115], 0\n\t"
    "v_mfma_f32_16x16x32_bf16 v[160:163], a[4:7], v[112:115], 0\n\t"
    "s_waitcnt lgkmcnt(1)\n\t"
    "v_lshlrev_b32 v228, 16, v120\n\t"
    "v_and_b32 v229, s69, v120\n\t"
    "v_lshlrev_b32 v230, 16, v121\n\t"
    "v_and_b32 v231, s69, v121\n\t"
    "v_pk_mul_f32 v[228:229], v[228:229], s[68:69] op_sel_hi:[1,0]\n\t"
    "v_pk_mul_f32 v[230:231], v[230:231], s[68:69] op_sel_hi:[1,0]\n\t"
    "v_cvt_pk_bf16_f32 v120, v228, v229\n\t"
    "v_cvt_pk_bf16_f32 v121, v230, v231\n\t"
    "v_lshlrev_b32 v232, 16, v122\n\t"
    "v_and_b32 v233, s69, v122\n\t"
    "v_lshlrev_b32 v234, 16, v123\n\t"
    "v_and_b32 v235, s69, v123\n\t"
    "v_pk_mul_f32 v[232:233], v[232:233], s[68:69] op_sel_hi:[1,0]\n\t"
    "v_pk_mul_f32 v[234:235], v[234:235], s[68:69] op_sel_hi:[1,0]\n\t"
    "v_cvt_pk_bf16_f32 v122, v232, v233\n\t"
    "v_cvt_pk_bf16_f32 v123, v234, v235\n\t"
    "v_mfma_f32_16x16x32_bf16 v[148:151], a[8:11], v[116:119], v[148:151]\n\t"
    "v_mfma_f32_16x16x32_bf16 v[160:163], a[12:15], v[116:119], v[160:163]\n\t"
    "s_waitcnt lgkmcnt(0)\n\t"
    "v_lshlrev_b32 v228, 16, v124\n\t"
    "v_and_b32 v229, s69, v124\n\t"
    "v_lshlrev_b32 v230, 16, v125\n\t"
    "v_and_b32 v231, s69, v125\n\t"
    "v_pk_mul_f32 v[228:229], v[228:229], s[68:69] op_sel_hi:[1,0]\n\t"
    "v_pk_mul_f32 v[230:231], v[230:231], s[68:69] op_sel_hi:[1,0]\n\t"
    "v_cvt_pk_bf16_f32 v124, v228, v229\n\t"
    "v_cvt_pk_bf16_f32 v125, v230, v231\n\t"
    "v_lshlrev_b32 v232, 16, v126\n\t"
    "v_and_b32 v233, s69, v126\n\t"
    "v_lshlrev_b32 v234, 16, v127\n\t"
    "v_and_b32 v235, s69, v127\n\t"
    "v_pk_mul_f32 v[232:233], v[232:233], s[68:69] op_sel_hi:[1,0]\n\t"
    "v_pk_mul_f32 v[234:235], v[234:235], s[68:69] op_sel_hi:[1,0]\n\t"
    "v_cvt_pk_bf16_f32 v126, v232, v233\n\t"
    "v_cvt_pk_bf16_f32 v127, v234, v235\n\t"
    "v_mfma_f32_16x16x32_bf16 v[148:151], a[16:19], v[120:123], v[148:151]\n\t"
    "v_mfma_f32_16x16x32_bf16 v[160:163], a[20:23], v[120:123], v[160:163]\n\t"
    "s_nop 1\n\t"
    "v_mfma_f32_16x16x32_bf16 v[148:151], a[24:27], v[124:127], v[148:151]\n\t"
    "v_mfma_f32_16x16x32_bf16 v[160:163], a[28:31], v[124:127], v[160:163]\n\t"
    "s_nop 7\n\t"
    "v_max3_f32 v242, v148, v149, v150\n\t"
    "v_max3_f32 v243, v151, v160, v161\n\t"
    "v_max3_f32 v242, v242, v162, v163\n\t"
    "v_max_f32 v242, v242, v243\n\t"
    "v_mov_b32 v243, v242\n\t"
    "s_nop 1\n\t"
    "v_permlane16_swap_b32 v242, v243\n\t"
    "s_nop 1\n\t"
    "v_max_f32 v242, v242, v243\n\t"
    "v_mov_b32 v243, v242\n\t"
    "s_nop 1\n\t"
    "v_permlane32_swap_b32 v242, v243\n\t"
    "s_nop 1\n\t"
    "v_max_f32 v242, v242, v243\n\t"
    "v_sub_f32 v196, 0, v242\n\t"
    "v_sub_f32 v197, 0, v242\n\t"
    "v_sub_f32 v198, 0, v242\n\t"
    "v_sub_f32 v199, 0, v242\n\t"
    "v_sub_f32 v148, v148, v242\n\t"
    "v_sub_f32 v149, v149, v242\n\t"
    "v_sub_f32 v150, v150, v242\n\t"
    "v_sub_f32 v151, v151, v242\n\t"
    "v_sub_f32 v160, v160, v242\n\t"
    "v_sub_f32 v161, v161, v242\n\t"
    "v_sub_f32 v162, v162, v242\n\t"
    "v_sub_f32 v163, v163, v242\n\t"
    "s_waitcnt vmcnt(8)\n\t"
    "ds_read_b128 v[128:131], v240 offset:4096\n\t"
    "ds_read_b128 v[132:135], v241 offset:4096\n\t"
    "ds_read_b128 v[136:139], v240 offset:10240\n\t"
    "ds_read_b128 v[140:143], v241 offset:10240\n\t"
    "s_add_u32 m0, s74, 65536\n\t"
    "s_nop 0\n\t"
    "buffer_load_dwordx4 v247, %[srdv], s62 offen nt lds\n\t"
    "s_add_u32 m0, s74, 73600\n\t"
    "s_nop 0\n\t"
    "buffer_load_dwordx4 v247, %[srdv], s62 offen offset:128 nt lds\n\t"
    "s_add_u32 s62, s62, 8192\n\t"
    "s_add_u32 m0, s74, 0\n\t"
    "s_nop 0\n\t"
    "buffer_load_dwordx4 v246, %[srdk], s62 offen nt lds\n\t"
    "s_add_u32 m0, s74, 8064\n\t"
    "s_nop 0\n\t"
    "buffer_load_dwordx4 v246, %[srdk], s62 offen offset:128 nt lds\n\t"
    "s_waitcnt lgkmcnt(3)\n\t"
    "v_lshlrev_b32 v228, 16, v128\n\t"
    "v_and_b32 v229, s69, v128\n\t"
    "v_lshlrev_b32 v230, 16, v129\n\t"
    "v_and_b32 v231, s69, v129\n\t"
    "v_pk_mul_f32 v[228:229], v[228:229], s[68:69] op_sel_hi:[1,0]\n\t"
    "v_pk_mul_f32 v[230:231], v[230:231], s[68:69] op_sel_hi:[1,0]\n\t"
    "v_cvt_pk_bf16_f32 v128, v228, v229\n\t"
    "v_cvt_pk_bf16_f32 v129, v230, v231\n\t"
    "v_lshlrev_b32 v232, 16, v130\n\t"
    "v_and_b32 v233, s69, v130\n\t"
    "v_lshlrev_b32 v234, 16, v131\n\t"
    "v_and_b32 v235, s69, v131\n\t"
    "v_pk_mul_f32 v[232:233], v[232:233], s[68:69] op_sel_hi:[1,0]\n\t"
    "v_pk_mul_f32 v[234:235], v[234:235], s[68:69] op_sel_hi:[1,0]\n\t"
    "v_cvt_pk_bf16_f32 v130, v232, v233\n\t"
    "v_cvt_pk_bf16_f32 v131, v234, v235\n\t"
    "s_waitcnt lgkmcnt(2)\n\t"
    "v_lshlrev_b32 v228, 16, v132\n\t"
    "v_and_b32 v229, s69, v132\n\t"
    "v_lshlrev_b32 v230, 16, v133\n\t"
    "v_and_b32 v231, s69, v133\n\t"
    "v_pk_mul_f32 v[228:229], v[228:229], s[68:69] op_sel_hi:[1,0]\n\t"
    "v_pk_mul_f32 v[230:231], v[230:231], s[68:69] op_sel_hi:[1,0]\n\t"
    "v_cvt_pk_bf16_f32 v132, v228, v229\n\t"
    "v_cvt_pk_bf16_f32 v133, v230, v231\n\t"
    "v_lshlrev_b32 v232, 16, v134\n\t"
    "v_and_b32 v233, s69, v134\n\t"
    "v_lshlrev_b32 v234, 16, v135\n\t"
    "v_and_b32 v235, s69, v135\n\t"
    "v_pk_mul_f32 v[232:233], v[232:233], s[68:69] op_sel_hi:[1,0]\n\t"
    "v_pk_mul_f32 v[234:235], v[234:235], s[68:69] op_sel_hi:[1,0]\n\t"
    "v_cvt_pk_bf16_f32 v134, v232, v233\n\t"
    "v_cvt_pk_bf16_f32 v135, v234, v235\n\t"
    "v_mfma_f32_16x16x32_bf16 v[152:155], a[0:3], v[128:131], 0\n\t"
    "v_mfma_f32_16x16x32_bf16 v[164:167], a[4:7], v[128:131], 0\n\t"
    "s_waitcnt lgkmcnt(1)\n\t"
    "v_lshlrev_b32 v228, 16, v136\n\t"
    "v_and_b32 v229, s69, v136\n\t"
    "v_lshlrev_b32 v230, 16, v137\n\t"
    "v_and_b32 v231, s69, v137\n\t"
    "v_pk_mul_f32 v[228:229], v[228:229], s[68:69] op_sel_hi:[1,0]\n\t"
    "v_pk_mul_f32 v[230:231], v[230:231], s[68:69] op_sel_hi:[1,0]\n\t"
    "v_cvt_pk_bf16_f32 v136, v228, v229\n\t"
    "v_cvt_pk_bf16_f32 v137, v230, v231\n\t"
    "v_lshlrev_b32 v232, 16, v138\n\t"
    "v_and_b32 v233, s69, v138\n\t"
    "v_lshlrev_b32 v234, 16, v139\n\t"
    "v_and_b32 v235, s69, v139\n\t"
    "v_pk_mul_f32 v[232:233], v[232:233], s[68:69] op_sel_hi:[1,0]\n\t"
    "v_pk_mul_f32 v[234:235], v[234:235], s[68:69] op_sel_hi:[1,0]\n\t"
    "v_cvt_pk_bf16_f32 v138, v232, v233\n\t"
    "v_cvt_pk_bf16_f32 v139, v234, v235\n\t"
    "v_mfma_f32_16x16x32_bf16 v[152:155], a[8:11], v[132:135], v[152:155]\n\t"
    "v_mfma_f32_16x16x32_bf16 v[164:167], a[12:15], v[132:135], v[164:167]\n\t"
    "s_waitcnt lgkmcnt(0)\n\t"
    "v_lshlrev_b32 v228, 16, v140\n\t"
    "v_and_b32 v229, s69, v140\n\t"
    "v_lshlrev_b32 v230, 16, v141\n\t"
    "v_and_b32 v231, s69, v141\n\t"
    "v_pk_mul_f32 v[228:229], v[228:229], s[68:69] op_sel_hi:[1,0]\n\t"
    "v_pk_mul_f32 v[230:231], v[230:231], s[68:69] op_sel_hi:[1,0]\n\t"
    "v_cvt_pk_bf16_f32 v140, v228, v229\n\t"
    "v_cvt_pk_bf16_f32 v141, v230, v231\n\t"
    "v_lshlrev_b32 v232, 16, v142\n\t"
    "v_and_b32 v233, s69, v142\n\t"
    "v_lshlrev_b32 v234, 16, v143\n\t"
    "v_and_b32 v235, s69, v143\n\t"
    "v_pk_mul_f32 v[232:233], v[232:233], s[68:69] op_sel_hi:[1,0]\n\t"
    "v_pk_mul_f32 v[234:235], v[234:235], s[68:69] op_sel_hi:[1,0]\n\t"
    "v_cvt_pk_bf16_f32 v142, v232, v233\n\t"
    "v_cvt_pk_bf16_f32 v143, v234, v235\n\t"
    "v_mfma_f32_16x16x32_bf16 v[152:155], a[16:19], v[136:139], v[152:155]\n\t"
    "v_mfma_f32_16x16x32_bf16 v[164:167], a[20:23], v[136:139], v[164:167]\n\t"
    "s_nop 1\n\t"
    "v_mfma_f32_16x16x32_bf16 v[152:155], a[24:27], v[140:143], v[152:155]\n\t"
    "v_mfma_f32_16x16x32_bf16 v[164:167], a[28:31], v[140:143], v[164:167]\n\t"
    "s_nop 7\n\t"
    "v_max3_f32 v242, v152, v153, v154\n\t"
    "v_max3_f32 v243, v155, v164, v165\n\t"
    "v_max3_f32 v242, v242, v166, v167\n\t"
    "v_max_f32 v242, v242, v243\n\t"
    "v_mov_b32 v243, v242\n\t"
    "s_nop 1\n\t"
    "v_permlane16_swap_b32 v242, v243\n\t"
    "s_nop 1\n\t"
    "v_max_f32 v242, v242, v243\n\t"
    "v_mov_b32 v243, v242\n\t"
    "s_nop 1\n\t"
    "v_permlane32_swap_b32 v242, v243\n\t"
    "s_nop 1\n\t"
    "v_max_f32 v242, v242, v243\n\t"
    "v_sub_f32 v200, 0, v242\n\t"
    "v_sub_f32 v201, 0, v242\n\t"
    "v_sub_f32 v202, 0, v242\n\t"
    "v_sub_f32 v203, 0, v242\n\t"
    "v_sub_f32 v152, v152, v242\n\t"
    "v_sub_f32 v153, v153, v242\n\t"
    "v_sub_f32 v154, v154, v242\n\t"
    "v_sub_f32 v155, v155, v242\n\t"
    "v_sub_f32 v164, v164, v242\n\t"
    "v_sub_f32 v165, v165, v242\n\t"
    "v_sub_f32 v166, v166, v242\n\t"
    "v_sub_f32 v167, v167, v242\n\t"
    "s_add_u32 m0, s74, 69632\n\t"
    "s_nop 0\n\t"
    "buffer_load_dwordx4 v247, %[srdv], s62 offen nt lds\n\t"
    "s_add_u32 m0, s74, 77696\n\t"
    "s_nop 0\n\t"
    "buffer_load_dwordx4 v247, %[srdv], s62 offen offset:128 nt lds\n\t"
    "s_add_u32 s62, s62, 8192\n\t"
    "s_add_u32 m0, s74, 4096\n\t"
    "s_nop 0\n\t"
    "buffer_load_dwordx4 v246, %[srdk], s62 offen nt lds\n\t"
    "s_add_u32 m0, s74, 12160\n\t"
    "s_nop 0\n\t"
    "buffer_load_dwordx4 v246, %[srdk], s62 offen offset:128 nt lds\n\t"
    "s_and_b32 s74, s62, 0xc000\n\t"
    "s_add_u32 s74, s74, %[mb]\n\t"
    "s_add_u32 m0, s74, 65536\n\t"
    "s_waitcnt vmcnt(12) lgkmcnt(0)\n\t"
    "s_branch .Lbar_%=\n\t"
    ".p2align 6\n\t"
    ".Ltop_%=:\n\t"
    "s_waitcnt lgkmcnt(6)\n\t"
    "v_mfma_f32_16x16x32_bf16 v[0:3], a[32:35], v[216:219], v[0:3]\n\t"
    "v_add_f32 v236, v236, v204\n\t"
    "v_add_f32 v237, v237, v208\n\t"
    "v_mfma_f32_16x16x32_bf16 v[4:7], a[32:35], v[220:223], v[4:7]\n\t"
    "v_add_f32 v238, v238, v212\n\t"
    "v_add_u32 v249, s67, v249\n\t"
    "v_mfma_f32_16x16x32_bf16 v[8:11], a[32:35], v[224:227], v[8:11]\n\t"
    "v_add_u32 v250, s67, v250\n\t"
    "v_add_u32 v251, s67, v251\n\t"
    "v_mfma_f32_16x16x32_bf16 v[20:23], a[36:39], v[224:227], v[20:23]\n\t"
    "s_waitcnt vmcnt(8)\n\t"
    ".Lbar_%=:\n\t"
    "s_barrier\n\t"
    "ds_read_b128 a[0:3], v244 offset:0\n\t"
    "ds_read_b128 a[4:7], v244 offset:2048\n\t"
    "v_mfma_f32_16x16x32_bf16 v[16:19], a[36:39], v[220:223], v[16:19]\n\t"
    "buffer_load_dwordx4 v247, %[srdv], s62 offen nt lds\n\t"
    "s_add_u32 m0, s74, 73600\n\t"
    "v_mfma_f32_16x16x32_bf16 v[12:15], a[36:39], v[216:219], v[12:15]\n\t"
    "buffer_load_dwordx4 v247, %[srdv], s62 offen offset:128 nt lds\n\t"
    "s_add_u32 m0, s74, 0\n\t"
    "s_add_u32 s62, s62, 8192\n\t"
    "v_mfma_f32_16x16x32_bf16 v[24:27], a[40:43], v[216:219], v[24:27]\n\t"
    "v_exp_f32 v228, v144\n\t"
    "v_mfma_f32_16x16x32_bf16 v[28:31], a[40:43], v[220:223], v[28:31]\n\t"
    "v_exp_f32 v229, v145\n\t"
    "v_mfma_f32_16x16x32_bf16 v[32:35], a[40:43], v[224:227], v[32:35]\n\t"
    "ds_read_b128 a[8:11], v245 offset:0\n\t"
    "ds_read_b128 a[12:15], v245 offset:2048\n\t"
    "v_mfma_f32_16x16x32_bf16 v[44:47], a[44:47], v[224:227], v[44:47]\n\t"
    "v_exp_f32 v230, v146\n\t"
    "v_mfma_f32_16x16x32_bf16 v[40:43], a[44:47], v[220:223], v[40:43]\n\t"
    "v_exp_f32 v231, v147\n\t"
    "v_mfma_f32_16x16x32_bf16 v[36:39], a[44:47], v[216:219], v[36:39]\n\t"
    "v_exp_f32 v232, v156\n\t"
    "v_mfma_f32_16x16x32_bf16 v[48:51], a[48:51], v[216:219], v[48:51]\n\t"
    "ds_read_b128 a[16:19], v244 offset:8192\n\t"
    "ds_read_b128 a[20:23], v244 offset:10240\n\t"
    "v_mfma_f32_16x16x32_bf16 v[52:55], a[48:51], v[220:223], v[52:55]\n\t"
    "v_cvt_pk_bf16_f32 v204, v228, v229\n\t"
    "v_cvt_pk_bf16_f32 v205, v230, v231\n\t"
    "v_mfma_f32_16x16x32_bf16 v[56:59], a[48:51], v[224:227], v[56:59]\n\t"
    "v_exp_f32 v233, v157\n\t"
    "s_waitcnt lgkmcnt(6)\n\t"
    "v_mfma_f32_16x16x32_bf16 v[68:71], a[52:55], v[224:227], v[68:71]\n\t"
    "v_exp_f32 v234, v158\n\t"
    "v_mfma_f32_16x16x32_bf16 v[64:67], a[52:55], v[220:223], v[64:67]\n\t"
    "ds_read_b128 a[24:27], v245 offset:8192\n\t"
    "ds_read_b128 a[28:31], v245 offset:10240\n\t"
    "v_mfma_f32_16x16x32_bf16 v[60:63], a[52:55], v[216:219], v[60:63]\n\t"
    "v_exp_f32 v235, v159\n\t"
    "v_mfma_f32_16x16x32_bf16 v[72:75], a[56:59], v[216:219], v[72:75]\n\t"
    "v_cvt_pk_bf16_f32 v206, v232, v233\n\t"
    "v_cvt_pk_bf16_f32 v207, v234, v235\n\t"
    "v_mfma_f32_16x16x32_bf16 v[76:79], a[56:59], v[220:223], v[76:79]\n\t"
    "v_exp_f32 v228, v148\n\t"
    "v_mfma_f32_16x16x32_bf16 v[80:83], a[56:59], v[224:227], v[80:83]\n\t"
    "ds_read_b64_tr_b16 a[32:33], v248 offset:0\n\t"
    "ds_read_b64_tr_b16 a[34:35], v248 offset:2048\n\t"
    "v_mfma_f32_16x16x32_bf16 v[92:95], a[60:63], v[224:227], v[92:95]\n\t"
    "v_exp_f32 v229, v149\n\t"
    "v_mfma_f32_16x16x32_bf16 v[88:91], a[60:63], v[220:223], v[88:91]\n\t"
    "v_exp_f32 v230, v150\n\t"
    "v_mfma_f32_16x16x32_bf16 v[84:87], a[60:63], v[216:219], v[84:87]\n\t"
    "v_exp_f32 v231, v151\n\t"
    "s_waitcnt lgkmcnt(2)\n\t"
    "v_mfma_f32_16x16x32_bf16 v[176:179], a[0:3], v[128:131], v[200:203]\n\t"
    "ds_read_b64_tr_b16 a[36:37], v249 offset:0\n\t"
    "ds_read_b64_tr_b16 a[38:39], v249 offset:2048\n\t"
    "v_mfma_f32_16x16x32_bf16 v[172:175], a[0:3], v[112:115], v[196:199]\n\t"
    "v_exp_f32 v232, v160\n\t"
    "v_mfma_f32_16x16x32_bf16 v[168:171], a[0:3], v[96:99], v[192:195]\n\t"
    "v_cvt_pk_bf16_f32 v208, v228, v229\n\t"
    "v_cvt_pk_bf16_f32 v209, v230, v231\n\t"
    "v_mfma_f32_16x16x32_bf16 v[216:219], a[64:67], v[204:207], 0\n\t"
    "v_exp_f32 v233, v161\n\t"
    "v_mfma_f32_16x16x32_bf16 v[180:183], a[4:7], v[96:99], v[192:195]\n\t"
    "ds_read_b64_tr_b16 a[40:41], v250 offset:0\n\t"
    "ds_read_b64_tr_b16 a[42:43], v250 offset:2048\n\t"
    "v_mfma_f32_16x16x32_bf16 v[184:187], a[4:7], v[112:115], v[196:199]\n\t"
    "v_exp_f32 v234, v162\n\t"
    "v_mfma_f32_16x16x32_bf16 v[188:191], a[4:7], v[128:131], v[200:203]\n\t"
    "v_exp_f32 v235, v163\n\t"
    "v_mfma_f32_16x16x32_bf16 v[176:179], a[8:11], v[132:135], v[176:179]\n\t"
    "v_cvt_pk_bf16_f32 v210, v232, v233\n\t"
    "v_cvt_pk_bf16_f32 v211, v234, v235\n\t"
    "v_mfma_f32_16x16x32_bf16 v[172:175], a[8:11], v[116:119], v[172:175]\n\t"
    "ds_read_b64_tr_b16 a[44:45], v251 offset:0\n\t"
    "ds_read_b64_tr_b16 a[46:47], v251 offset:2048\n\t"
    "v_mfma_f32_16x16x32_bf16 v[168:171], a[8:11], v[100:103], v[168:171]\n\t"
    "v_exp_f32 v228, v152\n\t"
    "v_mfma_f32_16x16x32_bf16 v[180:183], a[12:15], v[100:103], v[180:183]\n\t"
    "v_exp_f32 v229, v153\n\t"
    "v_mfma_f32_16x16x32_bf16 v[220:223], a[64:67], v[208:211], 0\n\t"
    "v_exp_f32 v230, v154\n\t"
    "v_mfma_f32_16x16x32_bf16 v[184:187], a[12:15], v[116:119], v[184:187]\n\t"
    "ds_read_b64_tr_b16 a[48:49], v248 offset:8192\n\t"
    "ds_read_b64_tr_b16 a[50:51], v248 offset:10240\n\t"
    "v_mfma_f32_16x16x32_bf16 v[188:191], a[12:15], v[132:135], v[188:191]\n\t"
    "v_exp_f32 v231, v155\n\t"
    "v_mfma_f32_16x16x32_bf16 v[176:179], a[16:19], v[136:139], v[176:179]\n\t"
    "v_exp_f32 v232, v164\n\t"
    "v_mfma_f32_16x16x32_bf16 v[172:175], a[16:19], v[120:123], v[172:175]\n\t"
    "v_cvt_pk_bf16_f32 v212, v228, v229\n\t"
    "v_cvt_pk_bf16_f32 v213, v230, v231\n\t"
    "v_mfma_f32_16x16x32_bf16 v[168:171], a[16:19], v[104:107], v[168:171]\n\t"
    "v_exp_f32 v233, v165\n\t"
    "v_mfma_f32_16x16x32_bf16 v[180:183], a[20:23], v[104:107], v[180:183]\n\t"
    "v_exp_f32 v234, v166\n\t"
    "v_mfma_f32_16x16x32_bf16 v[184:187], a[20:23], v[120:123], v[184:187]\n\t"
    "v_exp_f32 v235, v167\n\t"
    "v_mfma_f32_16x16x32_bf16 v[188:191], a[20:23], v[136:139], v[188:191]\n\t"
    "v_cvt_pk_bf16_f32 v214, v232, v233\n\t"
    "v_cvt_pk_bf16_f32 v215, v234, v235\n\t"
    "v_mfma_f32_16x16x32_bf16 v[176:179], a[24:27], v[140:143], v[176:179]\n\t"
    "ds_read_b64_tr_b16 a[52:53], v249 offset:8192\n\t"
    "ds_read_b64_tr_b16 a[54:55], v249 offset:10240\n\t"
    "v_mfma_f32_16x16x32_bf16 v[224:227], a[64:67], v[212:215], 0\n\t"
    "ds_read_b64_tr_b16 a[56:57], v250 offset:8192\n\t"
    "ds_read_b64_tr_b16 a[58:59], v250 offset:10240\n\t"
    "v_mfma_f32_16x16x32_bf16 v[172:175], a[24:27], v[124:127], v[172:175]\n\t"
    "ds_read_b64_tr_b16 a[60:61], v251 offset:8192\n\t"
    "ds_read_b64_tr_b16 a[62:63], v251 offset:10240\n\t"
    "v_mfma_f32_16x16x32_bf16 v[168:171], a[24:27], v[108:111], v[168:171]\n\t"
    "buffer_load_dwordx4 v246, %[srdk], s62 offen nt lds\n\t"
    "s_add_u32 m0, s74, 8064\n\t"
    "v_mfma_f32_16x16x32_bf16 v[180:183], a[28:31], v[108:111], v[180:183]\n\t"
    "buffer_load_dwordx4 v246, %[srdk], s62 offen offset:128 nt lds\n\t"
    "s_add_u32 m0, s74, 69632\n\t"
    "v_mfma_f32_16x16x32_bf16 v[184:187], a[28:31], v[124:127], v[184:187]\n\t"
    "v_max3_f32 v242, v216, v220, v224\n\t"
    "v_cmp_nge_f32 vcc, s65, v242\n\t"
    "v_mfma_f32_16x16x32_bf16 v[188:191], a[28:31], v[140:143], v[188:191]\n\t"
    "s_cmp_eq_u32 s74, s63\n\t"
    "s_cselect_b32 s67, 0xffff4000, s75\n\t"
    "s_cbranch_vccnz .Lmid_%=\n\t"
    ".Lback0_%=:\n\t"
    "s_waitcnt lgkmcnt(6)\n\t"
    "v_mfma_f32_16x16x32_bf16 v[0:3], a[32:35], v[204:207], v[0:3]\n\t"
    "v_add_f32 v236, v236, v216\n\t"
    "v_add_f32 v237, v237, v220\n\t"
    "v_mfma_f32_16x16x32_bf16 v[4:7], a[32:35], v[208:211], v[4:7]\n\t"
    "v_add_f32 v238, v238, v224\n\t"
    "v_mfma_f32_16x16x32_bf16 v[8:11], a[32:35], v[212:215], v[8:11]\n\t"
    "v_exp_f32 v228, v168\n\t"
    "v_mfma_f32_16x16x32_bf16 v[20:23], a[36:39], v[212:215], v[20:23]\n\t"
    "ds_read_b128 a[0:3], v244 offset:4096\n\t"
    "ds_read_b128 a[4:7], v244 offset:6144\n\t"
    "v_mfma_f32_16x16x32_bf16 v[16:19], a[36:39], v[208:211], v[16:19]\n\t"
    "buffer_load_dwordx4 v247, %[srdv], s62 offen nt lds\n\t"
    "s_add_u32 m0, s74, 77696\n\t"
    "v_mfma_f32_16x16x32_bf16 v[12:15], a[36:39], v[204:207], v[12:15]\n\t"
    "buffer_load_dwordx4 v247, %[srdv], s62 offen offset:128 nt lds\n\t"
    "s_add_u32 m0, s74, 4096\n\t"
    "s_add_u32 s62, s62, 8192\n\t"
    "v_mfma_f32_16x16x32_bf16 v[24:27], a[40:43], v[204:207], v[24:27]\n\t"
    "v_exp_f32 v229, v169\n\t"
    "v_mfma_f32_16x16x32_bf16 v[28:31], a[40:43], v[208:211], v[28:31]\n\t"
    "v_exp_f32 v230, v170\n\t"
    "v_mfma_f32_16x16x32_bf16 v[32:35], a[40:43], v[212:215], v[32:35]\n\t"
    "ds_read_b128 a[8:11], v245 offset:4096\n\t"
    "ds_read_b128 a[12:15], v245 offset:6144\n\t"
    "v_mfma_f32_16x16x32_bf16 v[44:47], a[44:47], v[212:215], v[44:47]\n\t"
    "v_exp_f32 v231, v171\n\t"
    "v_mfma_f32_16x16x32_bf16 v[40:43], a[44:47], v[208:211], v[40:43]\n\t"
    "v_exp_f32 v232, v180\n\t"
    "v_mfma_f32_16x16x32_bf16 v[36:39], a[44:47], v[204:207], v[36:39]\n\t"
    "v_cvt_pk_bf16_f32 v216, v228, v229\n\t"
    "v_cvt_pk_bf16_f32 v217, v230, v231\n\t"
    "v_mfma_f32_16x16x32_bf16 v[48:51], a[48:51], v[204:207], v[48:51]\n\t"
    "ds_read_b128 a[16:19], v244 offset:12288\n\t"
    "ds_read_b128 a[20:23], v244 offset:14336\n\t"
    "v_mfma_f32_16x16x32_bf16 v[52:55], a[48:51], v[208:211], v[52:55]\n\t"
    "v_exp_f32 v233, v181\n\t"
    "v_mfma_f32_16x16x32_bf16 v[56:59], a[48:51], v[212:215], v[56:59]\n\t"
    "v_exp_f32 v234, v182\n\t"
    "s_waitcnt lgkmcnt(6)\n\t"
    "v_mfma_f32_16x16x32_bf16 v[68:71], a[52:55], v[212:215], v[68:71]\n\t"
    "v_exp_f32 v235, v183\n\t"
    "v_mfma_f32_16x16x32_bf16 v[64:67], a[52:55], v[208:211], v[64:67]\n\t"
    "ds_read_b128 a[24:27], v245 offset:12288\n\t"
    "ds_read_b128 a[28:31], v245 offset:14336\n\t"
    "v_mfma_f32_16x16x32_bf16 v[60:63], a[52:55], v[204:207], v[60:63]\n\t"
    "v_cvt_pk_bf16_f32 v218, v232, v233\n\t"
    "v_cvt_pk_bf16_f32 v219, v234, v235\n\t"
    "v_mfma_f32_16x16x32_bf16 v[72:75], a[56:59], v[204:207], v[72:75]\n\t"
    "v_exp_f32 v228, v172\n\t"
    "v_mfma_f32_16x16x32_bf16 v[76:79], a[56:59], v[208:211], v[76:79]\n\t"
    "v_exp_f32 v229, v173\n\t"
    "v_mfma_f32_16x16x32_bf16 v[80:83], a[56:59], v[212:215], v[80:83]\n\t"
    "ds_read_b64_tr_b16 a[32:33], v248 offset:4096\n\t"
    "ds_read_b64_tr_b16 a[34:35], v248 offset:6144\n\t"
    "v_mfma_f32_16x16x32_bf16 v[92:95], a[60:63], v[212:215], v[92:95]\n\t"
    "v_exp_f32 v230, v174\n\t"
    "v_mfma_f32_16x16x32_bf16 v[88:91], a[60:63], v[208:211], v[88:91]\n\t"
    "v_exp_f32 v231, v175\n\t"
    "v_mfma_f32_16x16x32_bf16 v[84:87], a[60:63], v[204:207], v[84:87]\n\t"
    "v_exp_f32 v232, v184\n\t"
    "s_waitcnt lgkmcnt(2)\n\t"
    "v_mfma_f32_16x16x32_bf16 v[152:155], a[0:3], v[128:131], v[200:203]\n\t"
    "ds_read_b64_tr_b16 a[36:37], v249 offset:4096\n\t"
    "ds_read_b64_tr_b16 a[38:39], v249 offset:6144\n\t"
    "v_mfma_f32_16x16x32_bf16 v[148:151], a[0:3], v[112:115], v[196:199]\n\t"
    "v_cvt_pk_bf16_f32 v220, v228, v229\n\t"
    "v_cvt_pk_bf16_f32 v221, v230, v231\n\t"
    "v_mfma_f32_16x16x32_bf16 v[144:147], a[0:3], v[96:99], v[192:195]\n\t"
    "v_exp_f32 v233, v185\n\t"
    "v_mfma_f32_16x16x32_bf16 v[204:207], a[64:67], v[216:219], 0\n\t"
    "v_exp_f32 v234, v186\n\t"
    "v_mfma_f32_16x16x32_bf16 v[156:159], a[4:7], v[96:99], v[192:195]\n\t"
    "ds_read_b64_tr_b16 a[40:41], v250 offset:4096\n\t"
    "ds_read_b64_tr_b16 a[42:43], v250 offset:6144\n\t"
    "v_mfma_f32_16x16x32_bf16 v[160:163], a[4:7], v[112:115], v[196:199]\n\t"
    "v_exp_f32 v235, v187\n\t"
    "v_mfma_f32_16x16x32_bf16 v[164:167], a[4:7], v[128:131], v[200:203]\n\t"
    "v_cvt_pk_bf16_f32 v222, v232, v233\n\t"
    "v_cvt_pk_bf16_f32 v223, v234, v235\n\t"
    "v_mfma_f32_16x16x32_bf16 v[152:155], a[8:11], v[132:135], v[152:155]\n\t"
    "v_add_u32 v244, s67, v244\n\t"
    "v_add_u32 v245, s67, v245\n\t"
    "v_mfma_f32_16x16x32_bf16 v[148:151], a[8:11], v[116:119], v[148:151]\n\t"
    "ds_read_b64_tr_b16 a[44:45], v251 offset:4096\n\t"
    "ds_read_b64_tr_b16 a[46:47], v251 offset:6144\n\t"
    "v_mfma_f32_16x16x32_bf16 v[144:147], a[8:11], v[100:103], v[144:147]\n\t"
    "v_exp_f32 v228, v176\n\t"
    "v_mfma_f32_16x16x32_bf16 v[156:159], a[12:15], v[100:103], v[156:159]\n\t"
    "v_exp_f32 v229, v177\n\t"
    "v_mfma_f32_16x16x32_bf16 v[208:211], a[64:67], v[220:223], 0\n\t"
    "v_exp_f32 v230, v178\n\t"
    "v_mfma_f32_16x16x32_bf16 v[160:163], a[12:15], v[116:119], v[160:163]\n\t"
    "ds_read_b64_tr_b16 a[48:49], v248 offset:12288\n\t"
    "ds_read_b64_tr_b16 a[50:51], v248 offset:14336\n\t"
    "v_mfma_f32_16x16x32_bf16 v[164:167], a[12:15], v[132:135], v[164:167]\n\t"
    "v_exp_f32 v231, v179\n\t"
    "v_mfma_f32_16x16x32_bf16 v[152:155], a[16:19], v[136:139], v[152:155]\n\t"
    "v_exp_f32 v232, v188\n\t"
    "v_mfma_f32_16x16x32_bf16 v[148:151], a[16:19], v[120:123], v[148:151]\n\t"
    "v_cvt_pk_bf16_f32 v224, v228, v229\n\t"
    "v_cvt_pk_bf16_f32 v225, v230, v231\n\t"
    "v_mfma_f32_16x16x32_bf16 v[144:147], a[16:19], v[104:107], v[144:147]\n\t"
    "v_exp_f32 v233, v189\n\t"
    "v_mfma_f32_16x16x32_bf16 v[156:159], a[20:23], v[104:107], v[156:159]\n\t"
    "v_exp_f32 v234, v190\n\t"
    "v_mfma_f32_16x16x32_bf16 v[160:163], a[20:23], v[120:123], v[160:163]\n\t"
    "v_exp_f32 v235, v191\n\t"
    "v_mfma_f32_16x16x32_bf16 v[164:167], a[20:23], v[136:139], v[164:167]\n\t"
    "v_cvt_pk_bf16_f32 v226, v232, v233\n\t"
    "v_cvt_pk_bf16_f32 v227, v234, v235\n\t"
    "v_mfma_f32_16x16x32_bf16 v[152:155], a[24:27], v[140:143], v[152:155]\n\t"
    "ds_read_b64_tr_b16 a[52:53], v249 offset:12288\n\t"
    "ds_read_b64_tr_b16 a[54:55], v249 offset:14336\n\t"
    "v_mfma_f32_16x16x32_bf16 v[212:215], a[64:67], v[224:227], 0\n\t"
    "ds_read_b64_tr_b16 a[56:57], v250 offset:12288\n\t"
    "ds_read_b64_tr_b16 a[58:59], v250 offset:14336\n\t"
    "v_mfma_f32_16x16x32_bf16 v[148:151], a[24:27], v[124:127], v[148:151]\n\t"
    "ds_read_b64_tr_b16 a[60:61], v251 offset:12288\n\t"
    "ds_read_b64_tr_b16 a[62:63], v251 offset:14336\n\t"
    "v_mfma_f32_16x16x32_bf16 v[144:147], a[24:27], v[108:111], v[144:147]\n\t"
    "buffer_load_dwordx4 v246, %[srdk], s62 offen nt lds\n\t"
    "s_add_u32 m0, s74, 12160\n\t"
    "s_and_b32 s74, s62, 0xc000\n\t"
    "v_mfma_f32_16x16x32_bf16 v[156:159], a[28:31], v[108:111], v[156:159]\n\t"
    "buffer_load_dwordx4 v246, %[srdk], s62 offen offset:128 nt lds\n\t"
    "s_add_u32 s74, s74, %[mb]\n\t"
    "v_add_u32 v248, s67, v248\n\t"
    "v_mfma_f32_16x16x32_bf16 v[160:163], a[28:31], v[124:127], v[160:163]\n\t"
    "v_max3_f32 v242, v204, v208, v212\n\t"
    "v_cmp_nge_f32 vcc, s60, v242\n\t"
    "v_mfma_f32_16x16x32_bf16 v[164:167], a[28:31], v[140:143], v[164:167]\n\t"
    "s_add_u32 m0, s74, 65536\n\t"
    "s_cmp_lt_u32 s62, s76\n\t"
    "s_cbranch_vccnz .Lrare1_%=\n\t"
    ".Lback1_%=:\n\t"
    "s_cbranch_scc1 .Ltop_%=\n\t"
    "s_cmp_ge_u32 s62, 278528\n\t"
    "s_cbranch_scc1 .Ltail_%=\n\t"
    "s_cmp_lg_u64 s[84:85], 0\n\t"
    "s_cbranch_scc1 .Lrmp_%=\n\t"
    "s_add_u32 s78, s77, s92\n\t"
    "s_sub_i32 s77, s77, 1\n\t"
    "s_max_i32 s77, s77, 0\n\t"
    "s_or_b32 s66, s77, s92\n\t"
    "s_cselect_b32 s76, 0, 278528\n\t"
    ".Lpl_%=:\n\t"
    "s_sub_u32 s78, s78, 1\n\t"
    "s_cbranch_scc1 .Ltop_%=\n\t"
    "s_sleep 1\n\t"
    "s_branch .Lpl_%=\n\t"
    ".Lrmp_%=:\n\t"
    "s_memtime s[96:97]\n\t"
    "s_memrealtime s[80:81]\n\t"
    "s_and_b32 s78, s84, 0xff\n\t"
    "s_lshr_b64 s[84:85], s[84:85], 8\n\t"
    "s_and_b32 s66, s86, 0xff\n\t"
    "s_lshr_b64 s[86:87], s[86:87], 8\n\t"
    "s_lshl_b32 s66, s66, 2\n\t"
    "s_add_u32 s66, s66, s88\n\t"
    "s_lshr_b32 s90, s78, 2\n\t"
    "s_waitcnt lgkmcnt(0)\n\t"
    "s_sub_i32 s66, s66, s80\n\t"
    "s_mul_i32 s66, s66, 5\n\t"
    "s_ashr_i32 s66, s66, 4\n\t"
    "s_max_i32 s66, s66, s90\n\t"
    "s_min_u32 s78, s78, s66\n\t"
    "s_sub_u32 s93, s96, s93\n\t"
    "s_sub_u32 s94, s80, s94\n\t"
    "s_mul_i32 s93, s93, 10\n\t"
    "s_mul_i32 s94, s94, %[thr]\n\t"
    "s_cmp_ge_u32 s62, 65536\n\t"
    "s_cselect_b32 s94, s94, 0\n\t"
    "s_cmp_lt_u32 s93, s94\n\t"
    "s_cselect_b64 s[84:85], 0, s[84:85]\n\t"
    "s_cselect_b32 s92, %[psd], s92\n\t"
    "s_mov_b32 s93, s96\n\t"
    "s_mov_b32 s94, s80\n\t"
    "s_cmp_lg_u64 s[84:85], 0\n\t"
    "s_cselect_b32 s65, s65, s60\n\t"
    "s_cselect_b32 s66, 1, s92\n\t"
    "s_or_b32 s66, s66, s77\n\t"
    "s_cselect_b32 s76, 0, 278528\n\t"
    "s_branch .Lpl_%=\n\t"
    ".Lmid_%=:\n\t"
    "s_cmp_eq_u32 s65, 0xbf800000\n\t"
    "s_cbranch_scc0 .Lrare0_%=\n\t"
    "s_memrealtime s[80:81]\n\t"
    "s_and_b32 s78, s84, 0xff\n\t"
    "s_lshr_b64 s[84:85], s[84:85], 8\n\t"
    "s_and_b32 s66, s86, 0xff\n\t"
    "s_lshr_b64 s[86:87], s[86:87], 8\n\t"
    "s_lshl_b32 s66, s66, 2\n\t"
    "s_add_u32 s66, s66, s88\n\t"
    "s_lshr_b32 s90, s78, 2\n\t"
    "s_waitcnt lgkmcnt(0)\n\t"
    "s_sub_i32 s66, s66, s80\n\t"
    "s_mul_i32 s66, s66, 5\n\t"
    "s_ashr_i32 s66, s66, 4\n\t"
    "s_max_i32 s66, s66, s90\n\t"
    "s_min_u32 s78, s78, s66\n\t"
    ".Lpm_%=:\n\t"
    "s_sub_u32 s78, s78, 1\n\t"
    "s_cbranch_scc1 .Lpe_%=\n\t"
    "s_sleep 1\n\t"
    "s_branch .Lpm_%=\n\t"
    ".Lpe_%=:\n\t"
    "s_cmp_eq_u32 s89, 0\n\t"
    "s_cbranch_scc1 .Lnb_%=\n\t"
    "s_mov_b32 s89, 0\n\t"
    "s_waitcnt vmcnt(12) lgkmcnt(0)\n\t"
    "s_barrier\n\t"
    ".Lnb_%=:\n\t"
    "v_cmp_lt_f32 vcc, s60, v242\n\t"
    "s_cbranch_vccnz .Lrare0_%=\n\t"
    "s_branch .Lback0_%=\n\t"
    ".p2align 5\n\t"
    ".Ltail_%=:\n\t"
    "v_add_u32 v249, s67, v249\n\t"
    "v_add_u32 v250, s67, v250\n\t"
    "v_add_u32 v251, s67, v251\n\t"
    "s_waitcnt vmcnt(8) lgkmcnt(0)\n\t"
    "s_barrier\n\t"
    "v_mfma_f32_16x16x32_bf16 v[0:3], a[32:35], v[216:219], v[0:3]\n\t"
    "ds_read_b128 a[0:3], v244 offset:0\n\t"
    "ds_read_b128 a[4:7], v244 offset:2048\n\t"
    "v_mfma_f32_16x16x32_bf16 v[12:15], a[36:39], v[216:219], v[12:15]\n\t"
    "ds_read_b128 a[8:11], v245 offset:0\n\t"
    "ds_read_b128 a[12:15], v245 offset:2048\n\t"
    "v_mfma_f32_16x16x32_bf16 v[24:27], a[40:43], v[216:219], v[24:27]\n\t"
    "ds_read_b128 a[16:19], v244 offset:8192\n\t"
    "ds_read_b128 a[20:23], v244 offset:10240\n\t"
    "v_mfma_f32_16x16x32_bf16 v[36:39], a[44:47], v[216:219], v[36:39]\n\t"
    "ds_read_b128 a[24:27], v245 offset:8192\n\t"
    "ds_read_b128 a[28:31], v245 offset:10240\n\t"
    "v_mfma_f32_16x16x32_bf16 v[48:51], a[48:51], v[216:219], v[48:51]\n\t"
    "v_add_f32 v236, v236, v204\n\t"
    "v_add_f32 v237, v237, v208\n\t"
    "v_mfma_f32_16x16x32_bf16 v[60:63], a[52:55], v[216:219], v[60:63]\n\t"
    "v_add_f32 v238, v238, v212\n\t"
    "v_mfma_f32_16x16x32_bf16 v[72:75], a[56:59], v[216:219], v[72:75]\n\t"
    "v_exp_f32 v228, v144\n\t"
    "s_waitcnt lgkmcnt(7)\n\t"
    "v_mfma_f32_16x16x32_bf16 v[168:171], a[0:3], v[96:99], v[192:195]\n\t"
    "ds_read_b64_tr_b16 a[68:69], v248 offset:0\n\t"
    "ds_read_b64_tr_b16 a[70:71], v248 offset:2048\n\t"
    "s_waitcnt lgkmcnt(7)\n\t"
    "v_mfma_f32_16x16x32_bf16 v[168:171], a[8:11], v[100:103], v[168:171]\n\t"
    "v_exp_f32 v229, v145\n\t"
    "ds_read_b64_tr_b16 a[100:101], v248 offset:4096\n\t"
    "ds_read_b64_tr_b16 a[102:103], v248 offset:6144\n\t"
    "s_waitcnt lgkmcnt(6)\n\t"
    "v_mfma_f32_16x16x32_bf16 v[168:171], a[16:19], v[104:107], v[168:171]\n\t"
    "v_exp_f32 v230, v146\n\t"
    "ds_read_b64_tr_b16 a[72:73], v249 offset:0\n\t"
    "ds_read_b64_tr_b16 a[74:75], v249 offset:2048\n\t"
    "s_waitcnt lgkmcnt(6)\n\t"
    "v_mfma_f32_16x16x32_bf16 v[168:171], a[24:27], v[108:111], v[168:171]\n\t"
    "v_exp_f32 v231, v147\n\t"
    "ds_read_b64_tr_b16 a[104:105], v249 offset:4096\n\t"
    "ds_read_b64_tr_b16 a[106:107], v249 offset:6144\n\t"
    "v_mfma_f32_16x16x32_bf16 v[180:183], a[4:7], v[96:99], v[192:195]\n\t"
    "v_exp_f32 v232, v156\n\t"
    "v_cvt_pk_bf16_f32 v204, v228, v229\n\t"
    "v_cvt_pk_bf16_f32 v205, v230, v231\n\t"
    "v_mfma_f32_16x16x32_bf16 v[180:183], a[12:15], v[100:103], v[180:183]\n\t"
    "v_exp_f32 v233, v157\n\t"
    "ds_read_b64_tr_b16 a[76:77], v250 offset:0\n\t"
    "ds_read_b64_tr_b16 a[78:79], v250 offset:2048\n\t"
    "v_mfma_f32_16x16x32_bf16 v[180:183], a[20:23], v[104:107], v[180:183]\n\t"
    "v_exp_f32 v234, v158\n\t"
    "ds_read_b64_tr_b16 a[108:109], v250 offset:4096\n\t"
    "ds_read_b64_tr_b16 a[110:111], v250 offset:6144\n\t"
    "v_mfma_f32_16x16x32_bf16 v[180:183], a[28:31], v[108:111], v[180:183]\n\t"
    "v_exp_f32 v235, v159\n\t"
    "v_cvt_pk_bf16_f32 v206, v232, v233\n\t"
    "v_cvt_pk_bf16_f32 v207, v234, v235\n\t"
    "v_mfma_f32_16x16x32_bf16 v[84:87], a[60:63], v[216:219], v[84:87]\n\t"
    "v_mbcnt_lo_u32_b32 v240, -1, 0\n\t"
    "v_mbcnt_hi_u32_b32 v240, -1, v240\n\t"
    "v_lshl_add_u32 v241, v240, 4, %[ob]\n\t"
    "s_mov_b32 s62, 0\n\t"
    "v_mfma_f32_16x16x32_bf16 v[192:195], a[64:67], v[204:207], 0\n\t"
    "s_mov_b32 s63, 4096\n\t"
    "s_mov_b32 s64, 8192\n\t"
    "v_exp_f32 v228, v168\n\t"
    "v_mfma_f32_16x16x32_bf16 v[172:175], a[0:3], v[112:115], v[196:199]\n\t"
    "ds_read_b64_tr_b16 a[80:81], v251 offset:0\n\t"
    "ds_read_b64_tr_b16 a[82:83], v251 offset:2048\n\t"
    "v_exp_f32 v229, v169\n\t"
    "v_mfma_f32_16x16x32_bf16 v[172:175], a[8:11], v[116:119], v[172:175]\n\t"
    "ds_read_b64_tr_b16 a[112:113], v251 offset:4096\n\t"
    "ds_read_b64_tr_b16 a[114:115], v251 offset:6144\n\t"
    "v_exp_f32 v230, v170\n\t"
    "v_mfma_f32_16x16x32_bf16 v[172:175], a[16:19], v[120:123], v[172:175]\n\t"
    "ds_read_b64_tr_b16 a[84:85], v248 offset:8192\n\t"
    "ds_read_b64_tr_b16 a[86:87], v248 offset:10240\n\t"
    "v_exp_f32 v231, v171\n\t"
    "v_mfma_f32_16x16x32_bf16 v[172:175], a[24:27], v[124:127], v[172:175]\n\t"
    "ds_read_b64_tr_b16 a[116:117], v248 offset:12288\n\t"
    "ds_read_b64_tr_b16 a[118:119], v248 offset:14336\n\t"
    "v_exp_f32 v232, v180\n\t"
    "v_mfma_f32_16x16x32_bf16 v[184:187], a[4:7], v[112:115], v[196:199]\n\t"
    "v_cvt_pk_bf16_f32 v216, v228, v229\n\t"
    "v_cvt_pk_bf16_f32 v217, v230, v231\n\t"
    "v_exp_f32 v233, v181\n\t"
    "v_mfma_f32_16x16x32_bf16 v[184:187], a[12:15], v[116:119], v[184:187]\n\t"
    "ds_read_b64_tr_b16 a[88:89], v249 offset:8192\n\t"
    "ds_read_b64_tr_b16 a[90:91], v249 offset:10240\n\t"
    "v_exp_f32 v234, v182\n\t"
    "v_mfma_f32_16x16x32_bf16 v[184:187], a[20:23], v[120:123], v[184:187]\n\t"
    "ds_read_b64_tr_b16 a[120:121], v249 offset:12288\n\t"
    "ds_read_b64_tr_b16 a[122:123], v249 offset:14336\n\t"
    "v_exp_f32 v235, v183\n\t"
    "v_mfma_f32_16x16x32_bf16 v[184:187], a[28:31], v[124:127], v[184:187]\n\t"
    "v_cvt_pk_bf16_f32 v218, v232, v233\n\t"
    "v_cvt_pk_bf16_f32 v219, v234, v235\n\t"
    "v_exp_f32 v228, v148\n\t"
    "v_mfma_f32_16x16x32_bf16 v[4:7], a[32:35], v[220:223], v[4:7]\n\t"
    "ds_read_b64_tr_b16 a[92:93], v250 offset:8192\n\t"
    "ds_read_b64_tr_b16 a[94:95], v250 offset:10240\n\t"
    "v_mfma_f32_16x16x32_bf16 v[192:195], a[64:67], v[216:219], v[192:195]\n\t"
    "v_exp_f32 v229, v149\n\t"
    "v_mfma_f32_16x16x32_bf16 v[16:19], a[36:39], v[220:223], v[16:19]\n\t"
    "ds_read_b64_tr_b16 a[124:125], v250 offset:12288\n\t"
    "ds_read_b64_tr_b16 a[126:127], v250 offset:14336\n\t"
    "v_exp_f32 v230, v150\n\t"
    "v_mfma_f32_16x16x32_bf16 v[28:31], a[40:43], v[220:223], v[28:31]\n\t"
    "ds_read_b64_tr_b16 a[96:97], v251 offset:8192\n\t"
    "ds_read_b64_tr_b16 a[98:99], v251 offset:10240\n\t"
    "v_exp_f32 v231, v151\n\t"
    "v_mfma_f32_16x16x32_bf16 v[40:43], a[44:47], v[220:223], v[40:43]\n\t"
    "ds_read_b64_tr_b16 a[128:129], v251 offset:12288\n\t"
    "ds_read_b64_tr_b16 a[130:131], v251 offset:14336\n\t"
    "v_exp_f32 v232, v160\n\t"
    "v_mfma_f32_16x16x32_bf16 v[52:55], a[48:51], v[220:223], v[52:55]\n\t"
    "v_cmp_lt_f32 vcc, s60, v192\n\t"
    "s_cbranch_vccnz .LrareT0_%=\n\t"
    ".LbackT0_%=:\n\t"
    "v_add_f32 v236, v236, v192\n\t"
    "v_mfma_f32_16x16x32_bf16 v[64:67], a[52:55], v[220:223], v[64:67]\n\t"
    "v_rcp_f32 v236, v236\n\t"
    "v_cvt_pk_bf16_f32 v208, v228, v229\n\t"
    "v_cvt_pk_bf16_f32 v209, v230, v231\n\t"
    "v_mfma_f32_16x16x32_bf16 v[76:79], a[56:59], v[220:223], v[76:79]\n\t"
    "v_exp_f32 v233, v161\n\t"
    "v_exp_f32 v234, v162\n\t"
    "v_mfma_f32_16x16x32_bf16 v[88:91], a[60:63], v[220:223], v[88:91]\n\t"
    "v_exp_f32 v235, v163\n\t"
    "v_cvt_pk_bf16_f32 v210, v232, v233\n\t"
    "v_cvt_pk_bf16_f32 v211, v234, v235\n\t"
    "s_waitcnt lgkmcnt(2)\n\t"
    "v_mfma_f32_16x16x32_bf16 v[0:3], a[68:71], v[204:207], v[0:3]\n\t"
    "v_exp_f32 v228, v172\n\t"
    "v_exp_f32 v229, v173\n\t"
    "v_mfma_f32_16x16x32_bf16 v[0:3], a[100:103], v[216:219], v[0:3]\n\t"
    "v_exp_f32 v230, v174\n\t"
    "v_exp_f32 v231, v175\n\t"
    "v_mfma_f32_16x16x32_bf16 v[12:15], a[72:75], v[204:207], v[12:15]\n\t"
    "v_exp_f32 v232, v184\n\t"
    "v_cvt_pk_bf16_f32 v220, v228, v229\n\t"
    "v_cvt_pk_bf16_f32 v221, v230, v231\n\t"
    "v_mfma_f32_16x16x32_bf16 v[12:15], a[104:107], v[216:219], v[12:15]\n\t"
    "v_exp_f32 v233, v185\n\t"
    "v_exp_f32 v234, v186\n\t"
    "v_mfma_f32_16x16x32_bf16 v[196:199], a[64:67], v[208:211], 0\n\t"
    "v_exp_f32 v235, v187\n\t"
    "v_cvt_pk_bf16_f32 v222, v232, v233\n\t"
    "v_cvt_pk_bf16_f32 v223, v234, v235\n\t"
    "v_mfma_f32_16x16x32_bf16 v[24:27], a[76:79], v[204:207], v[24:27]\n\t"
    "v_and_b32 v104, 15, v240\n\t"
    "v_lshl_add_u32 v104, v104, 8, %[sg]\n\t"
    "v_lshrrev_b32 v105, 5, v240\n\t"
    "v_xor_b32 v105, v105, v240\n\t"
    "v_mfma_f32_16x16x32_bf16 v[196:199], a[64:67], v[220:223], v[196:199]\n\t"
    "v_and_b32 v105, 7, v105\n\t"
    "v_lshl_add_u32 v104, v105, 4, v104\n\t"
    "v_lshrrev_b32 v105, 1, v240\n\t"
    "v_xor_b32 v105, v105, v240\n\t"
    "v_mfma_f32_16x16x32_bf16 v[24:27], a[108:111], v[216:219], v[24:27]\n\t"
    "v_and_b32 v105, 8, v105\n\t"
    "v_add_u32 v104, v104, v105\n\t"
    "v_xor_b32 v105, 32, v104\n\t"
    "v_xor_b32 v106, 64, v104\n\t"
    "v_mfma_f32_16x16x32_bf16 v[36:39], a[80:83], v[204:207], v[36:39]\n\t"
    "v_xor_b32 v107, 0x60, v104\n\t"
    "v_lshrrev_b32 v108, 4, v240\n\t"
    "v_xor_b32 v108, v108, v240\n\t"
    "v_lshl_add_u32 v108, v108, 4, %[sg]\n\t"
    "v_mfma_f32_16x16x32_bf16 v[36:39], a[112:115], v[216:219], v[36:39]\n\t"
    "v_xor_b32 v109, 64, v108\n\t"
    "v_mul_f32 v0, v0, v236\n\t"
    "v_mul_f32 v1, v1, v236\n\t"
    "v_mul_f32 v2, v2, v236\n\t"
    "v_mfma_f32_16x16x32_bf16 v[48:51], a[84:87], v[204:207], v[48:51]\n\t"
    "v_mul_f32 v3, v3, v236\n\t"
    "v_cvt_pk_bf16_f32 v96, v0, v1\n\t"
    "v_cvt_pk_bf16_f32 v97, v2, v3\n\t"
    "v_mfma_f32_16x16x32_bf16 v[48:51], a[116:119], v[216:219], v[48:51]\n\t"
    "ds_write_b64 v104, v[96:97] offset:0\n\t"
    "v_mul_f32 v12, v12, v236\n\t"
    "v_cmp_lt_f32 vcc, s60, v196\n\t"
    "v_mfma_f32_16x16x32_bf16 v[60:63], a[88:91], v[204:207], v[60:63]\n\t"
    "s_cbranch_vccnz .LrareT1_%=\n\t"
    ".LbackT1_%=:\n\t"
    "v_add_f32 v237, v237, v196\n\t"
    "v_rcp_f32 v237, v237\n\t"
    "v_mfma_f32_16x16x32_bf16 v[60:63], a[120:123], v[216:219], v[60:63]\n\t"
    "v_mul_f32 v13, v13, v236\n\t"
    "v_mul_f32 v14, v14, v236\n\t"
    "v_mul_f32 v15, v15, v236\n\t"
    "v_cvt_pk_bf16_f32 v98, v12, v13\n\t"
    "v_mfma_f32_16x16x32_bf16 v[72:75], a[92:95], v[204:207], v[72:75]\n\t"
    "v_cvt_pk_bf16_f32 v99, v14, v15\n\t"
    "ds_write_b64 v105, v[98:99] offset:0\n\t"
    "v_mul_f32 v24, v24, v236\n\t"
    "v_mfma_f32_16x16x32_bf16 v[72:75], a[124:127], v[216:219], v[72:75]\n\t"
    "v_mul_f32 v25, v25, v236\n\t"
    "v_mul_f32 v26, v26, v236\n\t"
    "v_mul_f32 v27, v27, v236\n\t"
    "v_cvt_pk_bf16_f32 v100, v24, v25\n\t"
    "v_mfma_f32_16x16x32_bf16 v[84:87], a[96:99], v[204:207], v[84:87]\n\t"
    "v_cvt_pk_bf16_f32 v101, v26, v27\n\t"
    "ds_write_b64 v106, v[100:101] offset:0\n\t"
    "v_mul_f32 v36, v36, v236\n\t"
    "s_waitcnt lgkmcnt(2)\n\t"
    "v_mfma_f32_16x16x32_bf16 v[84:87], a[128:131], v[216:219], v[84:87]\n\t"
    "v_mul_f32 v37, v37, v236\n\t"
    "v_mul_f32 v38, v38, v236\n\t"
    "v_mul_f32 v39, v39, v236\n\t"
    "v_cvt_pk_bf16_f32 v102, v36, v37\n\t"
    "v_mfma_f32_16x16x32_bf16 v[176:179], a[0:3], v[128:131], v[200:203]\n\t"
    "v_cvt_pk_bf16_f32 v103, v38, v39\n\t"
    "ds_write_b64 v107, v[102:103] offset:0\n\t"
    "v_mul_f32 v48, v48, v236\n\t"
    "v_mfma_f32_16x16x32_bf16 v[176:179], a[8:11], v[132:135], v[176:179]\n\t"
    "v_mul_f32 v49, v49, v236\n\t"
    "v_mul_f32 v50, v50, v236\n\t"
    "v_mul_f32 v51, v51, v236\n\t"
    "v_cvt_pk_bf16_f32 v96, v48, v49\n\t"
    "v_mfma_f32_16x16x32_bf16 v[176:179], a[16:19], v[136:139], v[176:179]\n\t"
    "v_cvt_pk_bf16_f32 v97, v50, v51\n\t"
    "ds_write_b64 v104, v[96:97] offset:128\n\t"
    "v_mul_f32 v60, v60, v236\n\t"
    "v_mfma_f32_16x16x32_bf16 v[176:179], a[24:27], v[140:143], v[176:179]\n\t"
    "v_mul_f32 v61, v61, v236\n\t"
    "v_mul_f32 v62, v62, v236\n\t"
    "v_mul_f32 v63, v63, v236\n\t"
    "v_cvt_pk_bf16_f32 v98, v60, v61\n\t"
    "v_mfma_f32_16x16x32_bf16 v[188:191], a[4:7], v[128:131], v[200:203]\n\t"
    "v_cvt_pk_bf16_f32 v99, v62, v63\n\t"
    "ds_write_b64 v105, v[98:99] offset:128\n\t"
    "v_mul_f32 v72, v72, v236\n\t"
    "v_mfma_f32_16x16x32_bf16 v[188:191], a[12:15], v[132:135], v[188:191]\n\t"
    "v_mul_f32 v73, v73, v236\n\t"
    "v_mul_f32 v74, v74, v236\n\t"
    "v_mul_f32 v75, v75, v236\n\t"
    "v_cvt_pk_bf16_f32 v100, v72, v73\n\t"
    "v_mfma_f32_16x16x32_bf16 v[188:191], a[20:23], v[136:139], v[188:191]\n\t"
    "v_cvt_pk_bf16_f32 v101, v74, v75\n\t"
    "ds_write_b64 v106, v[100:101] offset:128\n\t"
    "v_mul_f32 v84, v84, v236\n\t"
    "v_mfma_f32_16x16x32_bf16 v[188:191], a[28:31], v[140:143], v[188:191]\n\t"
    "v_mul_f32 v85, v85, v236\n\t"
    "v_mul_f32 v86, v86, v236\n\t"
    "v_mul_f32 v87, v87, v236\n\t"
    "v_cvt_pk_bf16_f32 v102, v84, v85\n\t"
    "v_mfma_f32_16x16x32_bf16 v[8:11], a[32:35], v[224:227], v[8:11]\n\t"
    "v_cvt_pk_bf16_f32 v103, v86, v87\n\t"
    "ds_write_b64 v107, v[102:103] offset:128\n\t"
    "ds_read_b128 v[0:3], v108 offset:0\n\t"
    "v_mfma_f32_16x16x32_bf16 v[20:23], a[36:39], v[224:227], v[20:23]\n\t"
    "ds_read_b128 v[24:27], v109 offset:1024\n\t"
    "ds_read_b64 v[48:49], v108 offset:2056\n\t"
    "ds_read_b64 v[50:51], v108 offset:2048\n\t"
    "ds_read_b64 v[72:73], v109 offset:3080\n\t"
    "v_mfma_f32_16x16x32_bf16 v[32:35], a[40:43], v[224:227], v[32:35]\n\t"
    "ds_read_b64 v[74:75], v109 offset:3072\n\t"
    "v_exp_f32 v228, v152\n\t"
    "v_mfma_f32_16x16x32_bf16 v[44:47], a[44:47], v[224:227], v[44:47]\n\t"
    "v_exp_f32 v229, v153\n\t"
    "v_exp_f32 v230, v154\n\t"
    "v_mfma_f32_16x16x32_bf16 v[56:59], a[48:51], v[224:227], v[56:59]\n\t"
    "v_exp_f32 v231, v155\n\t"
    "v_exp_f32 v232, v164\n\t"
    "v_mfma_f32_16x16x32_bf16 v[68:71], a[52:55], v[224:227], v[68:71]\n\t"
    "v_mfma_f32_16x16x32_bf16 v[80:83], a[56:59], v[224:227], v[80:83]\n\t"
    "s_waitcnt lgkmcnt(5)\n\t"
    "buffer_store_dwordx4 v[0:3], v241, %[srdo], s62 offen offset:0 sc1\n\t"
    "v_cvt_pk_bf16_f32 v212, v228, v229\n\t"
    "v_cvt_pk_bf16_f32 v213, v230, v231\n\t"
    "s_waitcnt lgkmcnt(4)\n\t"
    "buffer_store_dwordx4 v[24:27], v241, %[srdo], s62 offen offset:1024 sc1\n\t"
    "v_mfma_f32_16x16x32_bf16 v[92:95], a[60:63], v[224:227], v[92:95]\n\t"
    "v_exp_f32 v233, v165\n\t"
    "v_exp_f32 v234, v166\n\t"
    "v_mfma_f32_16x16x32_bf16 v[4:7], a[68:71], v[208:211], v[4:7]\n\t"
    "s_waitcnt lgkmcnt(0)\n\t"
    "buffer_store_dwordx4 v[48:51], v241, %[srdo], s62 offen offset:2048 sc1\n\t"
    "v_exp_f32 v235, v167\n\t"
    "v_cvt_pk_bf16_f32 v214, v232, v233\n\t"
    "v_mfma_f32_16x16x32_bf16 v[4:7], a[100:103], v[220:223], v[4:7]\n\t"
    "buffer_store_dwordx4 v[72:75], v241, %[srdo], s62 offen offset:3072 sc1\n\t"
    "v_cvt_pk_bf16_f32 v215, v234, v235\n\t"
    "v_exp_f32 v228, v176\n\t"
    "v_mfma_f32_16x16x32_bf16 v[16:19], a[72:75], v[208:211], v[16:19]\n\t"
    "v_exp_f32 v229, v177\n\t"
    "v_exp_f32 v230, v178\n\t"
    "v_mfma_f32_16x16x32_bf16 v[16:19], a[104:107], v[220:223], v[16:19]\n\t"
    "v_exp_f32 v231, v179\n\t"
    "v_exp_f32 v232, v188\n\t"
    "v_mfma_f32_16x16x32_bf16 v[200:203], a[64:67], v[212:215], 0\n\t"
    "v_cvt_pk_bf16_f32 v224, v228, v229\n\t"
    "v_cvt_pk_bf16_f32 v225, v230, v231\n\t"
    "v_exp_f32 v233, v189\n\t"
    "v_mfma_f32_16x16x32_bf16 v[28:31], a[76:79], v[208:211], v[28:31]\n\t"
    "v_exp_f32 v234, v190\n\t"
    "v_exp_f32 v235, v191\n\t"
    "v_mfma_f32_16x16x32_bf16 v[28:31], a[108:111], v[220:223], v[28:31]\n\t"
    "v_cvt_pk_bf16_f32 v226, v232, v233\n\t"
    "v_cvt_pk_bf16_f32 v227, v234, v235\n\t"
    "v_mfma_f32_16x16x32_bf16 v[40:43], a[80:83], v[208:211], v[40:43]\n\t"
    "v_mul_f32 v4, v4, v237\n\t"
    "v_mul_f32 v5, v5, v237\n\t"
    "v_mfma_f32_16x16x32_bf16 v[200:203], a[64:67], v[224:227], v[200:203]\n\t"
    "v_mul_f32 v6, v6, v237\n\t"
    "v_mul_f32 v7, v7, v237\n\t"
    "v_cvt_pk_bf16_f32 v96, v4, v5\n\t"
    "v_cvt_pk_bf16_f32 v97, v6, v7\n\t"
    "v_mfma_f32_16x16x32_bf16 v[40:43], a[112:115], v[220:223], v[40:43]\n\t"
    "ds_write_b64 v104, v[96:97] offset:4096\n\t"
    "v_mul_f32 v16, v16, v237\n\t"
    "v_mfma_f32_16x16x32_bf16 v[52:55], a[84:87], v[208:211], v[52:55]\n\t"
    "v_cmp_lt_f32 vcc, s60, v200\n\t"
    "s_cbranch_vccnz .LrareT2_%=\n\t"
    ".LbackT2_%=:\n\t"
    "v_add_f32 v238, v238, v200\n\t"
    "v_mfma_f32_16x16x32_bf16 v[52:55], a[116:119], v[220:223], v[52:55]\n\t"
    "v_rcp_f32 v238, v238\n\t"
    "v_mul_f32 v17, v17, v237\n\t"
    "v_mul_f32 v18, v18, v237\n\t"
    "v_mfma_f32_16x16x32_bf16 v[64:67], a[88:91], v[208:211], v[64:67]\n\t"
    "v_mul_f32 v19, v19, v237\n\t"
    "v_cvt_pk_bf16_f32 v98, v16, v17\n\t"
    "v_cvt_pk_bf16_f32 v99, v18, v19\n\t"
    "v_mfma_f32_16x16x32_bf16 v[64:67], a[120:123], v[220:223], v[64:67]\n\t"
    "ds_write_b64 v105, v[98:99] offset:4096\n\t"
    "v_mul_f32 v28, v28, v237\n\t"
    "v_mul_f32 v29, v29, v237\n\t"
    "v_mfma_f32_16x16x32_bf16 v[76:79], a[92:95], v[208:211], v[76:79]\n\t"
    "v_mul_f32 v30, v30, v237\n\t"
    "v_mul_f32 v31, v31, v237\n\t"
    "v_cvt_pk_bf16_f32 v100, v28, v29\n\t"
    "v_cvt_pk_bf16_f32 v101, v30, v31\n\t"
    "v_mfma_f32_16x16x32_bf16 v[76:79], a[124:127], v[220:223], v[76:79]\n\t"
    "ds_write_b64 v106, v[100:101] offset:4096\n\t"
    "v_mul_f32 v40, v40, v237\n\t"
    "v_mul_f32 v41, v41, v237\n\t"
    "v_mfma_f32_16x16x32_bf16 v[88:91], a[96:99], v[208:211], v[88:91]\n\t"
    "v_mul_f32 v42, v42, v237\n\t"
    "v_mul_f32 v43, v43, v237\n\t"
    "v_cvt_pk_bf16_f32 v102, v40, v41\n\t"
    "v_cvt_pk_bf16_f32 v103, v42, v43\n\t"
    "v_mfma_f32_16x16x32_bf16 v[88:91], a[128:131], v[220:223], v[88:91]\n\t"
    "ds_write_b64 v107, v[102:103] offset:4096\n\t"
    "v_mul_f32 v52, v52, v237\n\t"
    "v_mul_f32 v53, v53, v237\n\t"
    "v_mfma_f32_16x16x32_bf16 v[8:11], a[68:71], v[212:215], v[8:11]\n\t"
    "v_mul_f32 v54, v54, v237\n\t"
    "v_mul_f32 v55, v55, v237\n\t"
    "v_cvt_pk_bf16_f32 v96, v52, v53\n\t"
    "v_cvt_pk_bf16_f32 v97, v54, v55\n\t"
    "v_mfma_f32_16x16x32_bf16 v[8:11], a[100:103], v[224:227], v[8:11]\n\t"
    "ds_write_b64 v104, v[96:97] offset:4224\n\t"
    "v_mul_f32 v64, v64, v237\n\t"
    "v_mul_f32 v65, v65, v237\n\t"
    "v_mfma_f32_16x16x32_bf16 v[20:23], a[72:75], v[212:215], v[20:23]\n\t"
    "v_mul_f32 v66, v66, v237\n\t"
    "v_mul_f32 v67, v67, v237\n\t"
    "v_cvt_pk_bf16_f32 v98, v64, v65\n\t"
    "v_cvt_pk_bf16_f32 v99, v66, v67\n\t"
    "v_mfma_f32_16x16x32_bf16 v[20:23], a[104:107], v[224:227], v[20:23]\n\t"
    "ds_write_b64 v105, v[98:99] offset:4224\n\t"
    "v_mul_f32 v76, v76, v237\n\t"
    "v_mul_f32 v77, v77, v237\n\t"
    "v_mfma_f32_16x16x32_bf16 v[32:35], a[76:79], v[212:215], v[32:35]\n\t"
    "v_mul_f32 v78, v78, v237\n\t"
    "v_mul_f32 v79, v79, v237\n\t"
    "v_cvt_pk_bf16_f32 v100, v76, v77\n\t"
    "v_cvt_pk_bf16_f32 v101, v78, v79\n\t"
    "v_mfma_f32_16x16x32_bf16 v[32:35], a[108:111], v[224:227], v[32:35]\n\t"
    "ds_write_b64 v106, v[100:101] offset:4224\n\t"
    "v_mul_f32 v88, v88, v237\n\t"
    "v_mul_f32 v89, v89, v237\n\t"
    "v_mfma_f32_16x16x32_bf16 v[44:47], a[80:83], v[212:215], v[44:47]\n\t"
    "v_mul_f32 v90, v90, v237\n\t"
    "v_mul_f32 v91, v91, v237\n\t"
    "v_cvt_pk_bf16_f32 v102, v88, v89\n\t"
    "v_cvt_pk_bf16_f32 v103, v90, v91\n\t"
    "v_mfma_f32_16x16x32_bf16 v[44:47], a[112:115], v[224:227], v[44:47]\n\t"
    "ds_write_b64 v107, v[102:103] offset:4224\n\t"
    "ds_read_b128 v[4:7], v108 offset:4096\n\t"
    "ds_read_b128 v[28:31], v109 offset:5120\n\t"
    "v_mfma_f32_16x16x32_bf16 v[56:59], a[84:87], v[212:215], v[56:59]\n\t"
    "ds_read_b64 v[52:53], v108 offset:6152\n\t"
    "ds_read_b64 v[54:55], v108 offset:6144\n\t"
    "ds_read_b64 v[76:77], v109 offset:7176\n\t"
    "ds_read_b64 v[78:79], v109 offset:7168\n\t"
    "v_mfma_f32_16x16x32_bf16 v[56:59], a[116:119], v[224:227], v[56:59]\n\t"
    "v_pk_mul_f32 v[8:9], v[8:9], v[238:239] op_sel_hi:[1,0]\n\t"
    "v_pk_mul_f32 v[10:11], v[10:11], v[238:239] op_sel_hi:[1,0]\n\t"
    "v_cvt_pk_bf16_f32 v96, v8, v9\n\t"
    "v_cvt_pk_bf16_f32 v97, v10, v11\n\t"
    "v_mfma_f32_16x16x32_bf16 v[68:71], a[88:91], v[212:215], v[68:71]\n\t"
    "ds_write_b64 v104, v[96:97] offset:8192\n\t"
    "v_pk_mul_f32 v[20:21], v[20:21], v[238:239] op_sel_hi:[1,0]\n\t"
    "v_mfma_f32_16x16x32_bf16 v[68:71], a[120:123], v[224:227], v[68:71]\n\t"
    "v_mfma_f32_16x16x32_bf16 v[80:83], a[92:95], v[212:215], v[80:83]\n\t"
    "s_waitcnt lgkmcnt(6)\n\t"
    "buffer_store_dwordx4 v[4:7], v241, %[srdo], s63 offen offset:0 sc1\n\t"
    "v_pk_mul_f32 v[22:23], v[22:23], v[238:239] op_sel_hi:[1,0]\n\t"
    "v_cvt_pk_bf16_f32 v98, v20, v21\n\t"
    "s_waitcnt lgkmcnt(5)\n\t"
    "buffer_store_dwordx4 v[28:31], v241, %[srdo], s63 offen offset:1024 sc1\n\t"
    "v_mfma_f32_16x16x32_bf16 v[80:83], a[124:127], v[224:227], v[80:83]\n\t"
    "v_cvt_pk_bf16_f32 v99, v22, v23\n\t"
    "ds_write_b64 v105, v[98:99] offset:8192\n\t"
    "s_waitcnt lgkmcnt(2)\n\t"
    "buffer_store_dwordx4 v[52:55], v241, %[srdo], s63 offen offset:2048 sc1\n\t"
    "v_mfma_f32_16x16x32_bf16 v[92:95], a[96:99], v[212:215], v[92:95]\n\t"
    "buffer_store_dwordx4 v[76:79], v241, %[srdo], s63 offen offset:3072 sc1\n\t"
    "v_pk_mul_f32 v[32:33], v[32:33], v[238:239] op_sel_hi:[1,0]\n\t"
    "v_pk_mul_f32 v[34:35], v[34:35], v[238:239] op_sel_hi:[1,0]\n\t"
    "v_cvt_pk_bf16_f32 v100, v32, v33\n\t"
    "v_mfma_f32_16x16x32_bf16 v[92:95], a[128:131], v[224:227], v[92:95]\n\t"
    "v_cvt_pk_bf16_f32 v101, v34, v35\n\t"
    "ds_write_b64 v106, v[100:101] offset:8192\n\t"
    "v_pk_mul_f32 v[44:45], v[44:45], v[238:239] op_sel_hi:[1,0]\n\t"
    "v_pk_mul_f32 v[46:47], v[46:47], v[238:239] op_sel_hi:[1,0]\n\t"
    "v_cvt_pk_bf16_f32 v102, v44, v45\n\t"
    "v_cvt_pk_bf16_f32 v103, v46, v47\n\t"
    "ds_write_b64 v107, v[102:103] offset:8192\n\t"
    "v_pk_mul_f32 v[56:57], v[56:57], v[238:239] op_sel_hi:[1,0]\n\t"
    "v_pk_mul_f32 v[58:59], v[58:59], v[238:239] op_sel_hi:[1,0]\n\t"
    "v_cvt_pk_bf16_f32 v96, v56, v57\n\t"
    "v_cvt_pk_bf16_f32 v97, v58, v59\n\t"
    "ds_write_b64 v104, v[96:97] offset:8320\n\t"
    "v_pk_mul_f32 v[68:69], v[68:69], v[238:239] op_sel_hi:[1,0]\n\t"
    "v_pk_mul_f32 v[70:71], v[70:71], v[238:239] op_sel_hi:[1,0]\n\t"
    "v_cvt_pk_bf16_f32 v98, v68, v69\n\t"
    "v_cvt_pk_bf16_f32 v99, v70, v71\n\t"
    "ds_write_b64 v105, v[98:99] offset:8320\n\t"
    "v_pk_mul_f32 v[80:81], v[80:81], v[238:239] op_sel_hi:[1,0]\n\t"
    "v_pk_mul_f32 v[82:83], v[82:83], v[238:239] op_sel_hi:[1,0]\n\t"
    "v_cvt_pk_bf16_f32 v100, v80, v81\n\t"
    "v_cvt_pk_bf16_f32 v101, v82, v83\n\t"
    "ds_write_b64 v106, v[100:101] offset:8320\n\t"
    "v_pk_mul_f32 v[92:93], v[92:93], v[238:239] op_sel_hi:[1,0]\n\t"
    "v_pk_mul_f32 v[94:95], v[94:95], v[238:239] op_sel_hi:[1,0]\n\t"
    "v_cvt_pk_bf16_f32 v102, v92, v93\n\t"
    "v_cvt_pk_bf16_f32 v103, v94, v95\n\t"
    "ds_write_b64 v107, v[102:103] offset:8320\n\t"
    "ds_read_b128 v[8:11], v108 offset:8192\n\t"
    "ds_read_b128 v[32:35], v109 offset:9216\n\t"
    "ds_read_b64 v[56:57], v108 offset:10248\n\t"
    "ds_read_b64 v[58:59], v108 offset:10240\n\t"
    "ds_read_b64 v[80:81], v109 offset:11272\n\t"
    "ds_read_b64 v[82:83], v109 offset:11264\n\t"
    "s_waitcnt lgkmcnt(5)\n\t"
    "buffer_store_dwordx4 v[8:11], v241, %[srdo], s64 offen offset:0 sc1\n\t"
    "s_waitcnt lgkmcnt(4)\n\t"
    "buffer_store_dwordx4 v[32:35], v241, %[srdo], s64 offen offset:1024 sc1\n\t"
    "s_waitcnt lgkmcnt(2)\n\t"
    "buffer_store_dwordx4 v[56:59], v241, %[srdo], s64 offen offset:2048 sc1\n\t"
    "s_waitcnt lgkmcnt(0)\n\t"
    "buffer_store_dwordx4 v[80:83], v241, %[srdo], s64 offen offset:3072 sc1\n\t"
    "s_nop 1\n\t"
    "s_endpgm\n\t"
    ".Lrare0_%=:\n\t"
    "s_nop 7\n\t"
    "v_max3_f32 v242, v144, v145, v146\n\t"
    "v_max3_f32 v243, v147, v156, v157\n\t"
    "v_max3_f32 v242, v242, v158, v159\n\t"
    "v_max_f32 v242, v242, v243\n\t"
    "v_mov_b32 v243, v242\n\t"
    "s_nop 1\n\t"
    "v_permlane16_swap_b32 v242, v243\n\t"
    "s_nop 1\n\t"
    "v_max_f32 v242, v242, v243\n\t"
    "v_mov_b32 v243, v242\n\t"
    "s_nop 1\n\t"
    "v_permlane32_swap_b32 v242, v243\n\t"
    "s_nop 1\n\t"
    "v_max_f32 v242, v242, v243\n\t"
    "v_max_f32 v242, 0, v242\n\t"
    "v_exp_f32 v252, -v242\n\t"
    "v_sub_f32 v192, v192, v242\n\t"
    "v_sub_f32 v193, v193, v242\n\t"
    "v_sub_f32 v194, v194, v242\n\t"
    "v_sub_f32 v195, v195, v242\n\t"
    "v_mul_f32 v236, v236, v252\n\t"
    "v_mul_f32 v0, v0, v252\n\t"
    "v_mul_f32 v1, v1, v252\n\t"
    "v_mul_f32 v2, v2, v252\n\t"
    "v_mul_f32 v3, v3, v252\n\t"
    "v_mul_f32 v12, v12, v252\n\t"
    "v_mul_f32 v13, v13, v252\n\t"
    "v_mul_f32 v14, v14, v252\n\t"
    "v_mul_f32 v15, v15, v252\n\t"
    "v_mul_f32 v24, v24, v252\n\t"
    "v_mul_f32 v25, v25, v252\n\t"
    "v_mul_f32 v26, v26, v252\n\t"
    "v_mul_f32 v27, v27, v252\n\t"
    "v_mul_f32 v36, v36, v252\n\t"
    "v_mul_f32 v37, v37, v252\n\t"
    "v_mul_f32 v38, v38, v252\n\t"
    "v_mul_f32 v39, v39, v252\n\t"
    "v_mul_f32 v48, v48, v252\n\t"
    "v_mul_f32 v49, v49, v252\n\t"
    "v_mul_f32 v50, v50, v252\n\t"
    "v_mul_f32 v51, v51, v252\n\t"
    "v_mul_f32 v60, v60, v252\n\t"
    "v_mul_f32 v61, v61, v252\n\t"
    "v_mul_f32 v62, v62, v252\n\t"
    "v_mul_f32 v63, v63, v252\n\t"
    "v_mul_f32 v72, v72, v252\n\t"
    "v_mul_f32 v73, v73, v252\n\t"
    "v_mul_f32 v74, v74, v252\n\t"
    "v_mul_f32 v75, v75, v252\n\t"
    "v_mul_f32 v84, v84, v252\n\t"
    "v_mul_f32 v85, v85, v252\n\t"
    "v_mul_f32 v86, v86, v252\n\t"
    "v_mul_f32 v87, v87, v252\n\t"
    "v_sub_f32 v144, v144, v242\n\t"
    "v_sub_f32 v145, v145, v242\n\t"
    "v_sub_f32 v146, v146, v242\n\t"
    "v_sub_f32 v147, v147, v242\n\t"
    "v_sub_f32 v156, v156, v242\n\t"
    "v_sub_f32 v157, v157, v242\n\t"
    "v_sub_f32 v158, v158, v242\n\t"
    "v_sub_f32 v159, v159, v242\n\t"
    "v_sub_f32 v168, v168, v242\n\t"
    "v_sub_f32 v169, v169, v242\n\t"
    "v_sub_f32 v170, v170, v242\n\t"
    "v_sub_f32 v171, v171, v242\n\t"
    "v_sub_f32 v180, v180, v242\n\t"
    "v_sub_f32 v181, v181, v242\n\t"
    "v_sub_f32 v182, v182, v242\n\t"
    "v_sub_f32 v183, v183, v242\n\t"
    "v_exp_f32 v228, v144\n\t"
    "s_nop 0\n\t"
    "v_exp_f32 v229, v145\n\t"
    "s_nop 0\n\t"
    "v_exp_f32 v230, v146\n\t"
    "s_nop 0\n\t"
    "v_exp_f32 v231, v147\n\t"
    "s_nop 0\n\t"
    "v_exp_f32 v232, v156\n\t"
    "s_nop 0\n\t"
    "v_cvt_pk_bf16_f32 v204, v228, v229\n\t"
    "s_nop 0\n\t"
    "v_cvt_pk_bf16_f32 v205, v230, v231\n\t"
    "s_nop 0\n\t"
    "v_exp_f32 v233, v157\n\t"
    "s_nop 0\n\t"
    "v_exp_f32 v234, v158\n\t"
    "s_nop 0\n\t"
    "v_exp_f32 v235, v159\n\t"
    "s_nop 0\n\t"
    "v_cvt_pk_bf16_f32 v206, v232, v233\n\t"
    "s_nop 0\n\t"
    "v_cvt_pk_bf16_f32 v207, v234, v235\n\t"
    "s_nop 0\n\t"
    "s_nop 1\n\t"
    "v_mfma_f32_16x16x32_bf16 v[216:219], a[64:67], v[204:207], 0\n\t"
    "v_max3_f32 v242, v148, v149, v150\n\t"
    "v_max3_f32 v243, v151, v160, v161\n\t"
    "v_max3_f32 v242, v242, v162, v163\n\t"
    "v_max_f32 v242, v242, v243\n\t"
    "v_mov_b32 v243, v242\n\t"
    "s_nop 1\n\t"
    "v_permlane16_swap_b32 v242, v243\n\t"
    "s_nop 1\n\t"
    "v_max_f32 v242, v242, v243\n\t"
    "v_mov_b32 v243, v242\n\t"
    "s_nop 1\n\t"
    "v_permlane32_swap_b32 v242, v243\n\t"
    "s_nop 1\n\t"
    "v_max_f32 v242, v242, v243\n\t"
    "v_max_f32 v242, 0, v242\n\t"
    "v_exp_f32 v252, -v242\n\t"
    "v_sub_f32 v196, v196, v242\n\t"
    "v_sub_f32 v197, v197, v242\n\t"
    "v_sub_f32 v198, v198, v242\n\t"
    "v_sub_f32 v199, v199, v242\n\t"
    "v_mul_f32 v237, v237, v252\n\t"
    "v_mul_f32 v4, v4, v252\n\t"
    "v_mul_f32 v5, v5, v252\n\t"
    "v_mul_f32 v6, v6, v252\n\t"
    "v_mul_f32 v7, v7, v252\n\t"
    "v_mul_f32 v16, v16, v252\n\t"
    "v_mul_f32 v17, v17, v252\n\t"
    "v_mul_f32 v18, v18, v252\n\t"
    "v_mul_f32 v19, v19, v252\n\t"
    "v_mul_f32 v28, v28, v252\n\t"
    "v_mul_f32 v29, v29, v252\n\t"
    "v_mul_f32 v30, v30, v252\n\t"
    "v_mul_f32 v31, v31, v252\n\t"
    "v_mul_f32 v40, v40, v252\n\t"
    "v_mul_f32 v41, v41, v252\n\t"
    "v_mul_f32 v42, v42, v252\n\t"
    "v_mul_f32 v43, v43, v252\n\t"
    "v_mul_f32 v52, v52, v252\n\t"
    "v_mul_f32 v53, v53, v252\n\t"
    "v_mul_f32 v54, v54, v252\n\t"
    "v_mul_f32 v55, v55, v252\n\t"
    "v_mul_f32 v64, v64, v252\n\t"
    "v_mul_f32 v65, v65, v252\n\t"
    "v_mul_f32 v66, v66, v252\n\t"
    "v_mul_f32 v67, v67, v252\n\t"
    "v_mul_f32 v76, v76, v252\n\t"
    "v_mul_f32 v77, v77, v252\n\t"
    "v_mul_f32 v78, v78, v252\n\t"
    "v_mul_f32 v79, v79, v252\n\t"
    "v_mul_f32 v88, v88, v252\n\t"
    "v_mul_f32 v89, v89, v252\n\t"
    "v_mul_f32 v90, v90, v252\n\t"
    "v_mul_f32 v91, v91, v252\n\t"
    "v_sub_f32 v148, v148, v242\n\t"
    "v_sub_f32 v149, v149, v242\n\t"
    "v_sub_f32 v150, v150, v242\n\t"
    "v_sub_f32 v151, v151, v242\n\t"
    "v_sub_f32 v160, v160, v242\n\t"
    "v_sub_f32 v161, v161, v242\n\t"
    "v_sub_f32 v162, v162, v242\n\t"
    "v_sub_f32 v163, v163, v242\n\t"
    "v_sub_f32 v172, v172, v242\n\t"
    "v_sub_f32 v173, v173, v242\n\t"
    "v_sub_f32 v174, v174, v242\n\t"
    "v_sub_f32 v175, v175, v242\n\t"
    "v_sub_f32 v184, v184, v242\n\t"
    "v_sub_f32 v185, v185, v242\n\t"
    "v_sub_f32 v186, v186, v242\n\t"
    "v_sub_f32 v187, v187, v242\n\t"
    "v_exp_f32 v228, v148\n\t"
    "s_nop 0\n\t"
    "v_exp_f32 v229, v149\n\t"
    "s_nop 0\n\t"
    "v_exp_f32 v230, v150\n\t"
    "s_nop 0\n\t"
    "v_exp_f32 v231, v151\n\t"
    "s_nop 0\n\t"
    "v_exp_f32 v232, v160\n\t"
    "s_nop 0\n\t"
    "v_cvt_pk_bf16_f32 v208, v228, v229\n\t"
    "s_nop 0\n\t"
    "v_cvt_pk_bf16_f32 v209, v230, v231\n\t"
    "s_nop 0\n\t"
    "v_exp_f32 v233, v161\n\t"
    "s_nop 0\n\t"
    "v_exp_f32 v234, v162\n\t"
    "s_nop 0\n\t"
    "v_exp_f32 v235, v163\n\t"
    "s_nop 0\n\t"
    "v_cvt_pk_bf16_f32 v210, v232, v233\n\t"
    "s_nop 0\n\t"
    "v_cvt_pk_bf16_f32 v211, v234, v235\n\t"
    "s_nop 0\n\t"
    "s_nop 1\n\t"
    "v_mfma_f32_16x16x32_bf16 v[220:223], a[64:67], v[208:211], 0\n\t"
    "v_max3_f32 v242, v152, v153, v154\n\t"
    "v_max3_f32 v243, v155, v164, v165\n\t"
    "v_max3_f32 v242, v242, v166, v167\n\t"
    "v_max_f32 v242, v242, v243\n\t"
    "v_mov_b32 v243, v242\n\t"
    "s_nop 1\n\t"
    "v_permlane16_swap_b32 v242, v243\n\t"
    "s_nop 1\n\t"
    "v_max_f32 v242, v242, v243\n\t"
    "v_mov_b32 v243, v242\n\t"
    "s_nop 1\n\t"
    "v_permlane32_swap_b32 v242, v243\n\t"
    "s_nop 1\n\t"
    "v_max_f32 v242, v242, v243\n\t"
    "v_max_f32 v242, 0, v242\n\t"
    "v_exp_f32 v252, -v242\n\t"
    "v_sub_f32 v200, v200, v242\n\t"
    "v_sub_f32 v201, v201, v242\n\t"
    "v_sub_f32 v202, v202, v242\n\t"
    "v_sub_f32 v203, v203, v242\n\t"
    "v_mul_f32 v238, v238, v252\n\t"
    "v_mul_f32 v8, v8, v252\n\t"
    "v_mul_f32 v9, v9, v252\n\t"
    "v_mul_f32 v10, v10, v252\n\t"
    "v_mul_f32 v11, v11, v252\n\t"
    "v_mul_f32 v20, v20, v252\n\t"
    "v_mul_f32 v21, v21, v252\n\t"
    "v_mul_f32 v22, v22, v252\n\t"
    "v_mul_f32 v23, v23, v252\n\t"
    "v_mul_f32 v32, v32, v252\n\t"
    "v_mul_f32 v33, v33, v252\n\t"
    "v_mul_f32 v34, v34, v252\n\t"
    "v_mul_f32 v35, v35, v252\n\t"
    "v_mul_f32 v44, v44, v252\n\t"
    "v_mul_f32 v45, v45, v252\n\t"
    "v_mul_f32 v46, v46, v252\n\t"
    "v_mul_f32 v47, v47, v252\n\t"
    "v_mul_f32 v56, v56, v252\n\t"
    "v_mul_f32 v57, v57, v252\n\t"
    "v_mul_f32 v58, v58, v252\n\t"
    "v_mul_f32 v59, v59, v252\n\t"
    "v_mul_f32 v68, v68, v252\n\t"
    "v_mul_f32 v69, v69, v252\n\t"
    "v_mul_f32 v70, v70, v252\n\t"
    "v_mul_f32 v71, v71, v252\n\t"
    "v_mul_f32 v80, v80, v252\n\t"
    "v_mul_f32 v81, v81, v252\n\t"
    "v_mul_f32 v82, v82, v252\n\t"
    "v_mul_f32 v83, v83, v252\n\t"
    "v_mul_f32 v92, v92, v252\n\t"
    "v_mul_f32 v93, v93, v252\n\t"
    "v_mul_f32 v94, v94, v252\n\t"
    "v_mul_f32 v95, v95, v252\n\t"
    "v_sub_f32 v152, v152, v242\n\t"
    "v_sub_f32 v153, v153, v242\n\t"
    "v_sub_f32 v154, v154, v242\n\t"
    "v_sub_f32 v155, v155, v242\n\t"
    "v_sub_f32 v164, v164, v242\n\t"
    "v_sub_f32 v165, v165, v242\n\t"
    "v_sub_f32 v166, v166, v242\n\t"
    "v_sub_f32 v167, v167, v242\n\t"
    "v_sub_f32 v176, v176, v242\n\t"
    "v_sub_f32 v177, v177, v242\n\t"
    "v_sub_f32 v178, v178, v242\n\t"
    "v_sub_f32 v179, v179, v242\n\t"
    "v_sub_f32 v188, v188, v242\n\t"
    "v_sub_f32 v189, v189, v242\n\t"
    "v_sub_f32 v190, v190, v242\n\t"
    "v_sub_f32 v191, v191, v242\n\t"
    "v_exp_f32 v228, v152\n\t"
    "s_nop 0\n\t"
    "v_exp_f32 v229, v153\n\t"
    "s_nop 0\n\t"
    "v_exp_f32 v230, v154\n\t"
    "s_nop 0\n\t"
    "v_exp_f32 v231, v155\n\t"
    "s_nop 0\n\t"
    "v_exp_f32 v232, v164\n\t"
    "s_nop 0\n\t"
    "v_cvt_pk_bf16_f32 v212, v228, v229\n\t"
    "s_nop 0\n\t"
    "v_cvt_pk_bf16_f32 v213, v230, v231\n\t"
    "s_nop 0\n\t"
    "v_exp_f32 v233, v165\n\t"
    "s_nop 0\n\t"
    "v_exp_f32 v234, v166\n\t"
    "s_nop 0\n\t"
    "v_exp_f32 v235, v167\n\t"
    "s_nop 0\n\t"
    "v_cvt_pk_bf16_f32 v214, v232, v233\n\t"
    "s_nop 0\n\t"
    "v_cvt_pk_bf16_f32 v215, v234, v235\n\t"
    "s_nop 0\n\t"
    "s_nop 1\n\t"
    "v_mfma_f32_16x16x32_bf16 v[224:227], a[64:67], v[212:215], 0\n\t"
    "s_nop 7\n\t"
    "s_branch .Lback0_%=\n\t"
    ".Lrare1_%=:\n\t"
    "s_nop 7\n\t"
    "v_max3_f32 v242, v168, v169, v170\n\t"
    "v_max3_f32 v243, v171, v180, v181\n\t"
    "v_max3_f32 v242, v242, v182, v183\n\t"
    "v_max_f32 v242, v242, v243\n\t"
    "v_mov_b32 v243, v242\n\t"
    "s_nop 1\n\t"
    "v_permlane16_swap_b32 v242, v243\n\t"
    "s_nop 1\n\t"
    "v_max_f32 v242, v242, v243\n\t"
    "v_mov_b32 v243, v242\n\t"
    "s_nop 1\n\t"
    "v_permlane32_swap_b32 v242, v243\n\t"
    "s_nop 1\n\t"
    "v_max_f32 v242, v242, v243\n\t"
    "v_max_f32 v242, 0, v242\n\t"
    "v_exp_f32 v252, -v242\n\t"
    "v_sub_f32 v192, v192, v242\n\t"
    "v_sub_f32 v193, v193, v242\n\t"
    "v_sub_f32 v194, v194, v242\n\t"
    "v_sub_f32 v195, v195, v242\n\t"
    "v_mul_f32 v236, v236, v252\n\t"
    "v_mul_f32 v0, v0, v252\n\t"
    "v_mul_f32 v1, v1, v252\n\t"
    "v_mul_f32 v2, v2, v252\n\t"
    "v_mul_f32 v3, v3, v252\n\t"
    "v_mul_f32 v12, v12, v252\n\t"
    "v_mul_f32 v13, v13, v252\n\t"
    "v_mul_f32 v14, v14, v252\n\t"
    "v_mul_f32 v15, v15, v252\n\t"
    "v_mul_f32 v24, v24, v252\n\t"
    "v_mul_f32 v25, v25, v252\n\t"
    "v_mul_f32 v26, v26, v252\n\t"
    "v_mul_f32 v27, v27, v252\n\t"
    "v_mul_f32 v36, v36, v252\n\t"
    "v_mul_f32 v37, v37, v252\n\t"
    "v_mul_f32 v38, v38, v252\n\t"
    "v_mul_f32 v39, v39, v252\n\t"
    "v_mul_f32 v48, v48, v252\n\t"
    "v_mul_f32 v49, v49, v252\n\t"
    "v_mul_f32 v50, v50, v252\n\t"
    "v_mul_f32 v51, v51, v252\n\t"
    "v_mul_f32 v60, v60, v252\n\t"
    "v_mul_f32 v61, v61, v252\n\t"
    "v_mul_f32 v62, v62, v252\n\t"
    "v_mul_f32 v63, v63, v252\n\t"
    "v_mul_f32 v72, v72, v252\n\t"
    "v_mul_f32 v73, v73, v252\n\t"
    "v_mul_f32 v74, v74, v252\n\t"
    "v_mul_f32 v75, v75, v252\n\t"
    "v_mul_f32 v84, v84, v252\n\t"
    "v_mul_f32 v85, v85, v252\n\t"
    "v_mul_f32 v86, v86, v252\n\t"
    "v_mul_f32 v87, v87, v252\n\t"
    "v_sub_f32 v144, v144, v242\n\t"
    "v_sub_f32 v145, v145, v242\n\t"
    "v_sub_f32 v146, v146, v242\n\t"
    "v_sub_f32 v147, v147, v242\n\t"
    "v_sub_f32 v156, v156, v242\n\t"
    "v_sub_f32 v157, v157, v242\n\t"
    "v_sub_f32 v158, v158, v242\n\t"
    "v_sub_f32 v159, v159, v242\n\t"
    "v_sub_f32 v168, v168, v242\n\t"
    "v_sub_f32 v169, v169, v242\n\t"
    "v_sub_f32 v170, v170, v242\n\t"
    "v_sub_f32 v171, v171, v242\n\t"
    "v_sub_f32 v180, v180, v242\n\t"
    "v_sub_f32 v181, v181, v242\n\t"
    "v_sub_f32 v182, v182, v242\n\t"
    "v_sub_f32 v183, v183, v242\n\t"
    "v_exp_f32 v228, v168\n\t"
    "s_nop 0\n\t"
    "v_exp_f32 v229, v169\n\t"
    "s_nop 0\n\t"
    "v_exp_f32 v230, v170\n\t"
    "s_nop 0\n\t"
    "v_exp_f32 v231, v171\n\t"
    "s_nop 0\n\t"
    "v_exp_f32 v232, v180\n\t"
    "s_nop 0\n\t"
    "v_cvt_pk_bf16_f32 v216, v228, v229\n\t"
    "s_nop 0\n\t"
    "v_cvt_pk_bf16_f32 v217, v230, v231\n\t"
    "s_nop 0\n\t"
    "v_exp_f32 v233, v181\n\t"
    "s_nop 0\n\t"
    "v_exp_f32 v234, v182\n\t"
    "s_nop 0\n\t"
    "v_exp_f32 v235, v183\n\t"
    "s_nop 0\n\t"
    "v_cvt_pk_bf16_f32 v218, v232, v233\n\t"
    "s_nop 0\n\t"
    "v_cvt_pk_bf16_f32 v219, v234, v235\n\t"
    "s_nop 0\n\t"
    "s_nop 1\n\t"
    "v_mfma_f32_16x16x32_bf16 v[204:207], a[64:67], v[216:219], 0\n\t"
    "v_max3_f32 v242, v172, v173, v174\n\t"
    "v_max3_f32 v243, v175, v184, v185\n\t"
    "v_max3_f32 v242, v242, v186, v187\n\t"
    "v_max_f32 v242, v242, v243\n\t"
    "v_mov_b32 v243, v242\n\t"
    "s_nop 1\n\t"
    "v_permlane16_swap_b32 v242, v243\n\t"
    "s_nop 1\n\t"
    "v_max_f32 v242, v242, v243\n\t"
    "v_mov_b32 v243, v242\n\t"
    "s_nop 1\n\t"
    "v_permlane32_swap_b32 v242, v243\n\t"
    "s_nop 1\n\t"
    "v_max_f32 v242, v242, v243\n\t"
    "v_max_f32 v242, 0, v242\n\t"
    "v_exp_f32 v252, -v242\n\t"
    "v_sub_f32 v196, v196, v242\n\t"
    "v_sub_f32 v197, v197, v242\n\t"
    "v_sub_f32 v198, v198, v242\n\t"
    "v_sub_f32 v199, v199, v242\n\t"
    "v_mul_f32 v237, v237, v252\n\t"
    "v_mul_f32 v4, v4, v252\n\t"
    "v_mul_f32 v5, v5, v252\n\t"
    "v_mul_f32 v6, v6, v252\n\t"
    "v_mul_f32 v7, v7, v252\n\t"
    "v_mul_f32 v16, v16, v252\n\t"
    "v_mul_f32 v17, v17, v252\n\t"
    "v_mul_f32 v18, v18, v252\n\t"
    "v_mul_f32 v19, v19, v252\n\t"
    "v_mul_f32 v28, v28, v252\n\t"
    "v_mul_f32 v29, v29, v252\n\t"
    "v_mul_f32 v30, v30, v252\n\t"
    "v_mul_f32 v31, v31, v252\n\t"
    "v_mul_f32 v40, v40, v252\n\t"
    "v_mul_f32 v41, v41, v252\n\t"
    "v_mul_f32 v42, v42, v252\n\t"
    "v_mul_f32 v43, v43, v252\n\t"
    "v_mul_f32 v52, v52, v252\n\t"
    "v_mul_f32 v53, v53, v252\n\t"
    "v_mul_f32 v54, v54, v252\n\t"
    "v_mul_f32 v55, v55, v252\n\t"
    "v_mul_f32 v64, v64, v252\n\t"
    "v_mul_f32 v65, v65, v252\n\t"
    "v_mul_f32 v66, v66, v252\n\t"
    "v_mul_f32 v67, v67, v252\n\t"
    "v_mul_f32 v76, v76, v252\n\t"
    "v_mul_f32 v77, v77, v252\n\t"
    "v_mul_f32 v78, v78, v252\n\t"
    "v_mul_f32 v79, v79, v252\n\t"
    "v_mul_f32 v88, v88, v252\n\t"
    "v_mul_f32 v89, v89, v252\n\t"
    "v_mul_f32 v90, v90, v252\n\t"
    "v_mul_f32 v91, v91, v252\n\t"
    "v_sub_f32 v148, v148, v242\n\t"
    "v_sub_f32 v149, v149, v242\n\t"
    "v_sub_f32 v150, v150, v242\n\t"
    "v_sub_f32 v151, v151, v242\n\t"
    "v_sub_f32 v160, v160, v242\n\t"
    "v_sub_f32 v161, v161, v242\n\t"
    "v_sub_f32 v162, v162, v242\n\t"
    "v_sub_f32 v163, v163, v242\n\t"
    "v_sub_f32 v172, v172, v242\n\t"
    "v_sub_f32 v173, v173, v242\n\t"
    "v_sub_f32 v174, v174, v242\n\t"
    "v_sub_f32 v175, v175, v242\n\t"
    "v_sub_f32 v184, v184, v242\n\t"
    "v_sub_f32 v185, v185, v242\n\t"
    "v_sub_f32 v186, v186, v242\n\t"
    "v_sub_f32 v187, v187, v242\n\t"
    "v_exp_f32 v228, v172\n\t"
    "s_nop 0\n\t"
    "v_exp_f32 v229, v173\n\t"
    "s_nop 0\n\t"
    "v_exp_f32 v230, v174\n\t"
    "s_nop 0\n\t"
    "v_exp_f32 v231, v175\n\t"
    "s_nop 0\n\t"
    "v_exp_f32 v232, v184\n\t"
    "s_nop 0\n\t"
    "v_cvt_pk_bf16_f32 v220, v228, v229\n\t"
    "s_nop 0\n\t"
    "v_cvt_pk_bf16_f32 v221, v230, v231\n\t"
    "s_nop 0\n\t"
    "v_exp_f32 v233, v185\n\t"
    "s_nop 0\n\t"
    "v_exp_f32 v234, v186\n\t"
    "s_nop 0\n\t"
    "v_exp_f32 v235, v187\n\t"
    "s_nop 0\n\t"
    "v_cvt_pk_bf16_f32 v222, v232, v233\n\t"
    "s_nop 0\n\t"
    "v_cvt_pk_bf16_f32 v223, v234, v235\n\t"
    "s_nop 0\n\t"
    "s_nop 1\n\t"
    "v_mfma_f32_16x16x32_bf16 v[208:211], a[64:67], v[220:223], 0\n\t"
    "v_max3_f32 v242, v176, v177, v178\n\t"
    "v_max3_f32 v243, v179, v188, v189\n\t"
    "v_max3_f32 v242, v242, v190, v191\n\t"
    "v_max_f32 v242, v242, v243\n\t"
    "v_mov_b32 v243, v242\n\t"
    "s_nop 1\n\t"
    "v_permlane16_swap_b32 v242, v243\n\t"
    "s_nop 1\n\t"
    "v_max_f32 v242, v242, v243\n\t"
    "v_mov_b32 v243, v242\n\t"
    "s_nop 1\n\t"
    "v_permlane32_swap_b32 v242, v243\n\t"
    "s_nop 1\n\t"
    "v_max_f32 v242, v242, v243\n\t"
    "v_max_f32 v242, 0, v242\n\t"
    "v_exp_f32 v252, -v242\n\t"
    "v_sub_f32 v200, v200, v242\n\t"
    "v_sub_f32 v201, v201, v242\n\t"
    "v_sub_f32 v202, v202, v242\n\t"
    "v_sub_f32 v203, v203, v242\n\t"
    "v_mul_f32 v238, v238, v252\n\t"
    "v_mul_f32 v8, v8, v252\n\t"
    "v_mul_f32 v9, v9, v252\n\t"
    "v_mul_f32 v10, v10, v252\n\t"
    "v_mul_f32 v11, v11, v252\n\t"
    "v_mul_f32 v20, v20, v252\n\t"
    "v_mul_f32 v21, v21, v252\n\t"
    "v_mul_f32 v22, v22, v252\n\t"
    "v_mul_f32 v23, v23, v252\n\t"
    "v_mul_f32 v32, v32, v252\n\t"
    "v_mul_f32 v33, v33, v252\n\t"
    "v_mul_f32 v34, v34, v252\n\t"
    "v_mul_f32 v35, v35, v252\n\t"
    "v_mul_f32 v44, v44, v252\n\t"
    "v_mul_f32 v45, v45, v252\n\t"
    "v_mul_f32 v46, v46, v252\n\t"
    "v_mul_f32 v47, v47, v252\n\t"
    "v_mul_f32 v56, v56, v252\n\t"
    "v_mul_f32 v57, v57, v252\n\t"
    "v_mul_f32 v58, v58, v252\n\t"
    "v_mul_f32 v59, v59, v252\n\t"
    "v_mul_f32 v68, v68, v252\n\t"
    "v_mul_f32 v69, v69, v252\n\t"
    "v_mul_f32 v70, v70, v252\n\t"
    "v_mul_f32 v71, v71, v252\n\t"
    "v_mul_f32 v80, v80, v252\n\t"
    "v_mul_f32 v81, v81, v252\n\t"
    "v_mul_f32 v82, v82, v252\n\t"
    "v_mul_f32 v83, v83, v252\n\t"
    "v_mul_f32 v92, v92, v252\n\t"
    "v_mul_f32 v93, v93, v252\n\t"
    "v_mul_f32 v94, v94, v252\n\t"
    "v_mul_f32 v95, v95, v252\n\t"
    "v_sub_f32 v152, v152, v242\n\t"
    "v_sub_f32 v153, v153, v242\n\t"
    "v_sub_f32 v154, v154, v242\n\t"
    "v_sub_f32 v155, v155, v242\n\t"
    "v_sub_f32 v164, v164, v242\n\t"
    "v_sub_f32 v165, v165, v242\n\t"
    "v_sub_f32 v166, v166, v242\n\t"
    "v_sub_f32 v167, v167, v242\n\t"
    "v_sub_f32 v176, v176, v242\n\t"
    "v_sub_f32 v177, v177, v242\n\t"
    "v_sub_f32 v178, v178, v242\n\t"
    "v_sub_f32 v179, v179, v242\n\t"
    "v_sub_f32 v188, v188, v242\n\t"
    "v_sub_f32 v189, v189, v242\n\t"
    "v_sub_f32 v190, v190, v242\n\t"
    "v_sub_f32 v191, v191, v242\n\t"
    "v_exp_f32 v228, v176\n\t"
    "s_nop 0\n\t"
    "v_exp_f32 v229, v177\n\t"
    "s_nop 0\n\t"
    "v_exp_f32 v230, v178\n\t"
    "s_nop 0\n\t"
    "v_exp_f32 v231, v179\n\t"
    "s_nop 0\n\t"
    "v_exp_f32 v232, v188\n\t"
    "s_nop 0\n\t"
    "v_cvt_pk_bf16_f32 v224, v228, v229\n\t"
    "s_nop 0\n\t"
    "v_cvt_pk_bf16_f32 v225, v230, v231\n\t"
    "s_nop 0\n\t"
    "v_exp_f32 v233, v189\n\t"
    "s_nop 0\n\t"
    "v_exp_f32 v234, v190\n\t"
    "s_nop 0\n\t"
    "v_exp_f32 v235, v191\n\t"
    "s_nop 0\n\t"
    "v_cvt_pk_bf16_f32 v226, v232, v233\n\t"
    "s_nop 0\n\t"
    "v_cvt_pk_bf16_f32 v227, v234, v235\n\t"
    "s_nop 0\n\t"
    "s_nop 1\n\t"
    "v_mfma_f32_16x16x32_bf16 v[212:215], a[64:67], v[224:227], 0\n\t"
    "s_nop 7\n\t"
    "s_branch .Lback1_%=\n\t"
    ".LrareT0_%=:\n\t"
    "s_nop 7\n\t"
    "v_max3_f32 v242, v144, v145, v146\n\t"
    "v_max3_f32 v243, v147, v156, v157\n\t"
    "v_max3_f32 v242, v242, v158, v159\n\t"
    "v_max_f32 v242, v242, v243\n\t"
    "v_mov_b32 v243, v242\n\t"
    "s_nop 1\n\t"
    "v_permlane16_swap_b32 v242, v243\n\t"
    "s_nop 1\n\t"
    "v_max_f32 v242, v242, v243\n\t"
    "v_mov_b32 v243, v242\n\t"
    "s_nop 1\n\t"
    "v_permlane32_swap_b32 v242, v243\n\t"
    "s_nop 1\n\t"
    "v_max_f32 v242, v242, v243\n\t"
    "v_mov_b32 v252, v242\n\t"
    "v_max3_f32 v242, v168, v169, v170\n\t"
    "v_max3_f32 v243, v171, v180, v181\n\t"
    "v_max3_f32 v242, v242, v182, v183\n\t"
    "v_max_f32 v242, v242, v243\n\t"
    "v_mov_b32 v243, v242\n\t"
    "s_nop 1\n\t"
    "v_permlane16_swap_b32 v242, v243\n\t"
    "s_nop 1\n\t"
    "v_max_f32 v242, v242, v243\n\t"
    "v_mov_b32 v243, v242\n\t"
    "s_nop 1\n\t"
    "v_permlane32_swap_b32 v242, v243\n\t"
    "s_nop 1\n\t"
    "v_max_f32 v242, v242, v243\n\t"
    "v_max3_f32 v242, 0, v242, v252\n\t"
    "v_exp_f32 v252, -v242\n\t"
    "v_sub_f32 v144, v144, v242\n\t"
    "v_sub_f32 v145, v145, v242\n\t"
    "v_sub_f32 v146, v146, v242\n\t"
    "v_sub_f32 v147, v147, v242\n\t"
    "v_sub_f32 v156, v156, v242\n\t"
    "v_sub_f32 v157, v157, v242\n\t"
    "v_sub_f32 v158, v158, v242\n\t"
    "v_sub_f32 v159, v159, v242\n\t"
    "v_sub_f32 v168, v168, v242\n\t"
    "v_sub_f32 v169, v169, v242\n\t"
    "v_sub_f32 v170, v170, v242\n\t"
    "v_sub_f32 v171, v171, v242\n\t"
    "v_sub_f32 v180, v180, v242\n\t"
    "v_sub_f32 v181, v181, v242\n\t"
    "v_sub_f32 v182, v182, v242\n\t"
    "v_sub_f32 v183, v183, v242\n\t"
    "v_mul_f32 v236, v236, v252\n\t"
    "v_mul_f32 v0, v0, v252\n\t"
    "v_mul_f32 v1, v1, v252\n\t"
    "v_mul_f32 v2, v2, v252\n\t"
    "v_mul_f32 v3, v3, v252\n\t"
    "v_mul_f32 v12, v12, v252\n\t"
    "v_mul_f32 v13, v13, v252\n\t"
    "v_mul_f32 v14, v14, v252\n\t"
    "v_mul_f32 v15, v15, v252\n\t"
    "v_mul_f32 v24, v24, v252\n\t"
    "v_mul_f32 v25, v25, v252\n\t"
    "v_mul_f32 v26, v26, v252\n\t"
    "v_mul_f32 v27, v27, v252\n\t"
    "v_mul_f32 v36, v36, v252\n\t"
    "v_mul_f32 v37, v37, v252\n\t"
    "v_mul_f32 v38, v38, v252\n\t"
    "v_mul_f32 v39, v39, v252\n\t"
    "v_mul_f32 v48, v48, v252\n\t"
    "v_mul_f32 v49, v49, v252\n\t"
    "v_mul_f32 v50, v50, v252\n\t"
    "v_mul_f32 v51, v51, v252\n\t"
    "v_mul_f32 v60, v60, v252\n\t"
    "v_mul_f32 v61, v61, v252\n\t"
    "v_mul_f32 v62, v62, v252\n\t"
    "v_mul_f32 v63, v63, v252\n\t"
    "v_mul_f32 v72, v72, v252\n\t"
    "v_mul_f32 v73, v73, v252\n\t"
    "v_mul_f32 v74, v74, v252\n\t"
    "v_mul_f32 v75, v75, v252\n\t"
    "v_mul_f32 v84, v84, v252\n\t"
    "v_mul_f32 v85, v85, v252\n\t"
    "v_mul_f32 v86, v86, v252\n\t"
    "v_mul_f32 v87, v87, v252\n\t"
    "v_exp_f32 v144, v144\n\t"
    "v_exp_f32 v145, v145\n\t"
    "v_exp_f32 v146, v146\n\t"
    "v_exp_f32 v147, v147\n\t"
    "v_exp_f32 v156, v156\n\t"
    "v_exp_f32 v157, v157\n\t"
    "v_exp_f32 v158, v158\n\t"
    "v_exp_f32 v159, v159\n\t"
    "v_exp_f32 v168, v168\n\t"
    "v_exp_f32 v169, v169\n\t"
    "v_exp_f32 v170, v170\n\t"
    "v_exp_f32 v171, v171\n\t"
    "v_exp_f32 v180, v180\n\t"
    "v_exp_f32 v181, v181\n\t"
    "v_exp_f32 v182, v182\n\t"
    "v_exp_f32 v183, v183\n\t"
    "s_nop 0\n\t"
    "v_cvt_pk_bf16_f32 v204, v144, v145\n\t"
    "v_cvt_pk_bf16_f32 v205, v146, v147\n\t"
    "v_cvt_pk_bf16_f32 v206, v156, v157\n\t"
    "v_cvt_pk_bf16_f32 v207, v158, v159\n\t"
    "v_cvt_pk_bf16_f32 v216, v168, v169\n\t"
    "v_cvt_pk_bf16_f32 v217, v170, v171\n\t"
    "v_cvt_pk_bf16_f32 v218, v180, v181\n\t"
    "v_cvt_pk_bf16_f32 v219, v182, v183\n\t"
    "s_nop 1\n\t"
    "v_mfma_f32_16x16x32_bf16 v[192:195], a[64:67], v[204:207], 0\n\t"
    "v_mfma_f32_16x16x32_bf16 v[192:195], a[64:67], v[216:219], v[192:195]\n\t"
    "s_nop 7\n\t"
    "s_branch .LbackT0_%=\n\t"
    ".LrareT1_%=:\n\t"
    "s_nop 7\n\t"
    "v_max3_f32 v242, v148, v149, v150\n\t"
    "v_max3_f32 v243, v151, v160, v161\n\t"
    "v_max3_f32 v242, v242, v162, v163\n\t"
    "v_max_f32 v242, v242, v243\n\t"
    "v_mov_b32 v243, v242\n\t"
    "s_nop 1\n\t"
    "v_permlane16_swap_b32 v242, v243\n\t"
    "s_nop 1\n\t"
    "v_max_f32 v242, v242, v243\n\t"
    "v_mov_b32 v243, v242\n\t"
    "s_nop 1\n\t"
    "v_permlane32_swap_b32 v242, v243\n\t"
    "s_nop 1\n\t"
    "v_max_f32 v242, v242, v243\n\t"
    "v_mov_b32 v252, v242\n\t"
    "v_max3_f32 v242, v172, v173, v174\n\t"
    "v_max3_f32 v243, v175, v184, v185\n\t"
    "v_max3_f32 v242, v242, v186, v187\n\t"
    "v_max_f32 v242, v242, v243\n\t"
    "v_mov_b32 v243, v242\n\t"
    "s_nop 1\n\t"
    "v_permlane16_swap_b32 v242, v243\n\t"
    "s_nop 1\n\t"
    "v_max_f32 v242, v242, v243\n\t"
    "v_mov_b32 v243, v242\n\t"
    "s_nop 1\n\t"
    "v_permlane32_swap_b32 v242, v243\n\t"
    "s_nop 1\n\t"
    "v_max_f32 v242, v242, v243\n\t"
    "v_max3_f32 v242, 0, v242, v252\n\t"
    "v_exp_f32 v252, -v242\n\t"
    "v_sub_f32 v148, v148, v242\n\t"
    "v_sub_f32 v149, v149, v242\n\t"
    "v_sub_f32 v150, v150, v242\n\t"
    "v_sub_f32 v151, v151, v242\n\t"
    "v_sub_f32 v160, v160, v242\n\t"
    "v_sub_f32 v161, v161, v242\n\t"
    "v_sub_f32 v162, v162, v242\n\t"
    "v_sub_f32 v163, v163, v242\n\t"
    "v_sub_f32 v172, v172, v242\n\t"
    "v_sub_f32 v173, v173, v242\n\t"
    "v_sub_f32 v174, v174, v242\n\t"
    "v_sub_f32 v175, v175, v242\n\t"
    "v_sub_f32 v184, v184, v242\n\t"
    "v_sub_f32 v185, v185, v242\n\t"
    "v_sub_f32 v186, v186, v242\n\t"
    "v_sub_f32 v187, v187, v242\n\t"
    "v_mul_f32 v237, v237, v252\n\t"
    "v_mul_f32 v4, v4, v252\n\t"
    "v_mul_f32 v5, v5, v252\n\t"
    "v_mul_f32 v6, v6, v252\n\t"
    "v_mul_f32 v7, v7, v252\n\t"
    "v_mul_f32 v16, v16, v252\n\t"
    "v_mul_f32 v17, v17, v252\n\t"
    "v_mul_f32 v18, v18, v252\n\t"
    "v_mul_f32 v19, v19, v252\n\t"
    "v_mul_f32 v28, v28, v252\n\t"
    "v_mul_f32 v29, v29, v252\n\t"
    "v_mul_f32 v30, v30, v252\n\t"
    "v_mul_f32 v31, v31, v252\n\t"
    "v_mul_f32 v40, v40, v252\n\t"
    "v_mul_f32 v41, v41, v252\n\t"
    "v_mul_f32 v42, v42, v252\n\t"
    "v_mul_f32 v43, v43, v252\n\t"
    "v_mul_f32 v52, v52, v252\n\t"
    "v_mul_f32 v53, v53, v252\n\t"
    "v_mul_f32 v54, v54, v252\n\t"
    "v_mul_f32 v55, v55, v252\n\t"
    "v_mul_f32 v64, v64, v252\n\t"
    "v_mul_f32 v65, v65, v252\n\t"
    "v_mul_f32 v66, v66, v252\n\t"
    "v_mul_f32 v67, v67, v252\n\t"
    "v_mul_f32 v76, v76, v252\n\t"
    "v_mul_f32 v77, v77, v252\n\t"
    "v_mul_f32 v78, v78, v252\n\t"
    "v_mul_f32 v79, v79, v252\n\t"
    "v_mul_f32 v88, v88, v252\n\t"
    "v_mul_f32 v89, v89, v252\n\t"
    "v_mul_f32 v90, v90, v252\n\t"
    "v_mul_f32 v91, v91, v252\n\t"
    "v_exp_f32 v148, v148\n\t"
    "v_exp_f32 v149, v149\n\t"
    "v_exp_f32 v150, v150\n\t"
    "v_exp_f32 v151, v151\n\t"
    "v_exp_f32 v160, v160\n\t"
    "v_exp_f32 v161, v161\n\t"
    "v_exp_f32 v162, v162\n\t"
    "v_exp_f32 v163, v163\n\t"
    "v_exp_f32 v172, v172\n\t"
    "v_exp_f32 v173, v173\n\t"
    "v_exp_f32 v174, v174\n\t"
    "v_exp_f32 v175, v175\n\t"
    "v_exp_f32 v184, v184\n\t"
    "v_exp_f32 v185, v185\n\t"
    "v_exp_f32 v186, v186\n\t"
    "v_exp_f32 v187, v187\n\t"
    "s_nop 0\n\t"
    "v_cvt_pk_bf16_f32 v208, v148, v149\n\t"
    "v_cvt_pk_bf16_f32 v209, v150, v151\n\t"
    "v_cvt_pk_bf16_f32 v210, v160, v161\n\t"
    "v_cvt_pk_bf16_f32 v211, v162, v163\n\t"
    "v_cvt_pk_bf16_f32 v220, v172, v173\n\t"
    "v_cvt_pk_bf16_f32 v221, v174, v175\n\t"
    "v_cvt_pk_bf16_f32 v222, v184, v185\n\t"
    "v_cvt_pk_bf16_f32 v223, v186, v187\n\t"
    "s_nop 1\n\t"
    "v_mfma_f32_16x16x32_bf16 v[196:199], a[64:67], v[208:211], 0\n\t"
    "v_mfma_f32_16x16x32_bf16 v[196:199], a[64:67], v[220:223], v[196:199]\n\t"
    "s_nop 7\n\t"
    "s_branch .LbackT1_%=\n\t"
    ".LrareT2_%=:\n\t"
    "s_nop 7\n\t"
    "v_max3_f32 v242, v152, v153, v154\n\t"
    "v_max3_f32 v243, v155, v164, v165\n\t"
    "v_max3_f32 v242, v242, v166, v167\n\t"
    "v_max_f32 v242, v242, v243\n\t"
    "v_mov_b32 v243, v242\n\t"
    "s_nop 1\n\t"
    "v_permlane16_swap_b32 v242, v243\n\t"
    "s_nop 1\n\t"
    "v_max_f32 v242, v242, v243\n\t"
    "v_mov_b32 v243, v242\n\t"
    "s_nop 1\n\t"
    "v_permlane32_swap_b32 v242, v243\n\t"
    "s_nop 1\n\t"
    "v_max_f32 v242, v242, v243\n\t"
    "v_mov_b32 v252, v242\n\t"
    "v_max3_f32 v242, v176, v177, v178\n\t"
    "v_max3_f32 v243, v179, v188, v189\n\t"
    "v_max3_f32 v242, v242, v190, v191\n\t"
    "v_max_f32 v242, v242, v243\n\t"
    "v_mov_b32 v243, v242\n\t"
    "s_nop 1\n\t"
    "v_permlane16_swap_b32 v242, v243\n\t"
    "s_nop 1\n\t"
    "v_max_f32 v242, v242, v243\n\t"
    "v_mov_b32 v243, v242\n\t"
    "s_nop 1\n\t"
    "v_permlane32_swap_b32 v242, v243\n\t"
    "s_nop 1\n\t"
    "v_max_f32 v242, v242, v243\n\t"
    "v_max3_f32 v242, 0, v242, v252\n\t"
    "v_exp_f32 v252, -v242\n\t"
    "v_sub_f32 v152, v152, v242\n\t"
    "v_sub_f32 v153, v153, v242\n\t"
    "v_sub_f32 v154, v154, v242\n\t"
    "v_sub_f32 v155, v155, v242\n\t"
    "v_sub_f32 v164, v164, v242\n\t"
    "v_sub_f32 v165, v165, v242\n\t"
    "v_sub_f32 v166, v166, v242\n\t"
    "v_sub_f32 v167, v167, v242\n\t"
    "v_sub_f32 v176, v176, v242\n\t"
    "v_sub_f32 v177, v177, v242\n\t"
    "v_sub_f32 v178, v178, v242\n\t"
    "v_sub_f32 v179, v179, v242\n\t"
    "v_sub_f32 v188, v188, v242\n\t"
    "v_sub_f32 v189, v189, v242\n\t"
    "v_sub_f32 v190, v190, v242\n\t"
    "v_sub_f32 v191, v191, v242\n\t"
    "v_mul_f32 v238, v238, v252\n\t"
    "v_mul_f32 v8, v8, v252\n\t"
    "v_mul_f32 v9, v9, v252\n\t"
    "v_mul_f32 v10, v10, v252\n\t"
    "v_mul_f32 v11, v11, v252\n\t"
    "v_mul_f32 v20, v20, v252\n\t"
    "v_mul_f32 v21, v21, v252\n\t"
    "v_mul_f32 v22, v22, v252\n\t"
    "v_mul_f32 v23, v23, v252\n\t"
    "v_mul_f32 v32, v32, v252\n\t"
    "v_mul_f32 v33, v33, v252\n\t"
    "v_mul_f32 v34, v34, v252\n\t"
    "v_mul_f32 v35, v35, v252\n\t"
    "v_mul_f32 v44, v44, v252\n\t"
    "v_mul_f32 v45, v45, v252\n\t"
    "v_mul_f32 v46, v46, v252\n\t"
    "v_mul_f32 v47, v47, v252\n\t"
    "v_mul_f32 v56, v56, v252\n\t"
    "v_mul_f32 v57, v57, v252\n\t"
    "v_mul_f32 v58, v58, v252\n\t"
    "v_mul_f32 v59, v59, v252\n\t"
    "v_mul_f32 v68, v68, v252\n\t"
    "v_mul_f32 v69, v69, v252\n\t"
    "v_mul_f32 v70, v70, v252\n\t"
    "v_mul_f32 v71, v71, v252\n\t"
    "v_mul_f32 v80, v80, v252\n\t"
    "v_mul_f32 v81, v81, v252\n\t"
    "v_mul_f32 v82, v82, v252\n\t"
    "v_mul_f32 v83, v83, v252\n\t"
    "v_mul_f32 v92, v92, v252\n\t"
    "v_mul_f32 v93, v93, v252\n\t"
    "v_mul_f32 v94, v94, v252\n\t"
    "v_mul_f32 v95, v95, v252\n\t"
    "v_exp_f32 v152, v152\n\t"
    "v_exp_f32 v153, v153\n\t"
    "v_exp_f32 v154, v154\n\t"
    "v_exp_f32 v155, v155\n\t"
    "v_exp_f32 v164, v164\n\t"
    "v_exp_f32 v165, v165\n\t"
    "v_exp_f32 v166, v166\n\t"
    "v_exp_f32 v167, v167\n\t"
    "v_exp_f32 v176, v176\n\t"
    "v_exp_f32 v177, v177\n\t"
    "v_exp_f32 v178, v178\n\t"
    "v_exp_f32 v179, v179\n\t"
    "v_exp_f32 v188, v188\n\t"
    "v_exp_f32 v189, v189\n\t"
    "v_exp_f32 v190, v190\n\t"
    "v_exp_f32 v191, v191\n\t"
    "s_nop 0\n\t"
    "v_cvt_pk_bf16_f32 v212, v152, v153\n\t"
    "v_cvt_pk_bf16_f32 v213, v154, v155\n\t"
    "v_cvt_pk_bf16_f32 v214, v164, v165\n\t"
    "v_cvt_pk_bf16_f32 v215, v166, v167\n\t"
    "v_cvt_pk_bf16_f32 v224, v176, v177\n\t"
    "v_cvt_pk_bf16_f32 v225, v178, v179\n\t"
    "v_cvt_pk_bf16_f32 v226, v188, v189\n\t"
    "v_cvt_pk_bf16_f32 v227, v190, v191\n\t"
    "s_nop 1\n\t"
    "v_mfma_f32_16x16x32_bf16 v[200:203], a[64:67], v[212:215], 0\n\t"
    "v_mfma_f32_16x16x32_bf16 v[200:203], a[64:67], v[224:227], v[200:203]\n\t"
    "s_nop 7\n\t"
    "s_branch .LbackT2_%=\n\t"
    ".Lend_%=:\n\t"
    : "+{v[244:247]}"(ka), "+{v[248:251]}"(va)
    : [mb] "s"(mb), [seq] "s"((!(xcc & 1u)) ? 2211992438536ull : 2211992438536ull), [dl] "s"(184264925797962ull), [pss] "s"((xcc & 1u) ? 2u : 4u), [p2] "s"(0u), [thr] "s"((xcc & 1u) ? 177u : 184u), [psd] "s"((xcc & 1u) ? 0u : 1u), [qo] "s"(QSTG - KRING + wid * 12288u), [sg] "s"(ldsb + (wid < 2u ? 131072u + wid * 12288u : KRING + 32768u + (wid - 2u) * 65536u)), [srdk] "s"(srdK), [srdv] "s"(srdV), [srdo] "s"(srdO), [ob] "s"(wid * 12288u)
    : "v0", "v1", "v2", "v3", "v4", "v5", "v6", "v7", "v8", "v9", "v10", "v11", "v12", "v13", "v14", "v15", "v16", "v17", "v18", "v19", "v20", "v21", "v22", "v23", "v24", "v25", "v26", "v27", "v28", "v29", "v30", "v31", "v32", "v33", "v34", "v35", "v36", "v37", "v38", "v39", "v40", "v41", "v42", "v43", "v44", "v45", "v46", "v47", "v48", "v49", "v50", "v51", "v52", "v53", "v54", "v55", "v56", "v57", "v58", "v59", "v60", "v61", "v62", "v63", "v64", "v65", "v66", "v67", "v68", "v69", "v70", "v71", "v72", "v73", "v74", "v75", "v76", "v77", "v78", "v79", "v80", "v81", "v82", "v83", "v84", "v85", "v86", "v87", "v88", "v89", "v90", "v91", "v92", "v93", "v94", "v95", "v96", "v97", "v98", "v99", "v100", "v101", "v102", "v103", "v104", "v105", "v106", "v107", "v108", "v109", "v110", "v111", "v112", "v113", "v114", "v115", "v116", "v117", "v118", "v119", "v120", "v121", "v122", "v123", "v124", "v125", "v126", "v127", "v128", "v129", "v130", "v131", "v132", "v133", "v134", "v135", "v136", "v137", "v138", "v139", "v140", "v141", "v142", "v143", "v144", "v145", "v146", "v147", "v148", "v149", "v150", "v151", "v152", "v153", "v154", "v155", "v156", "v157", "v158", "v159", "v160", "v161", "v162", "v163", "v164", "v165", "v166", "v167", "v168", "v169", "v170", "v171", "v172", "v173", "v174", "v175", "v176", "v177", "v178", "v179", "v180", "v181", "v182", "v183", "v184", "v185", "v186", "v187", "v188", "v189", "v190", "v191", "v192", "v193", "v194", "v195", "v196", "v197", "v198", "v199", "v200", "v201", "v202", "v203", "v204", "v205", "v206", "v207", "v208", "v209", "v210", "v211", "v212", "v213", "v214", "v215", "v216", "v217", "v218", "v219", "v220", "v221", "v222", "v223", "v224", "v225", "v226", "v227", "v228", "v229", "v230", "v231", "v232", "v233", "v234", "v235", "v236", "v237", "v238", "v239", "v240", "v241", "v242", "v243", "v252", "a0", "a1", "a2", "a3", "a4", "a5", "a6", "a7", "a8", "a9", "a10", "a11", "a12", "a13", "a14", "a15", "a16", "a17", "a18", "a19", "a20", "a21", "a22", "a23", "a24", "a25", "a26", "a27", "a28", "a29", "a30", "a31", "a32", "a33", "a34", "a35", "a36", "a37", "a38", "a39", "a40", "a41", "a42", "a43", "a44", "a45", "a46", "a47", "a48", "a49", "a50", "a51", "a52", "a53", "a54", "a55", "a56", "a57", "a58", "a59", "a60", "a61", "a62", "a63", "a64", "a65", "a66", "a67", "a68", "a69", "a70", "a71", "a72", "a73", "a74", "a75", "a76", "a77", "a78", "a79", "a80", "a81", "a82", "a83", "a84", "a85", "a86", "a87", "a88", "a89", "a90", "a91", "a92", "a93", "a94", "a95", "a96", "a97", "a98", "a99", "a100", "a101", "a102", "a103", "a104", "a105", "a106", "a107", "a108", "a109", "a110", "a111", "a112", "a113", "a114", "a115", "a116", "a117", "a118", "a119", "a120", "a121", "a122", "a123", "a124", "a125", "a126", "a127", "a128", "a129", "a130", "a131", "s60", "s61", "s62", "s63", "s64", "s65", "s66", "s67", "s68", "s69", "s70", "s71", "s72", "s73", "s74", "s75", "s76", "s77", "s78", "s79", "s80", "s81", "s82", "s83", "s84", "s85", "s86", "s87", "s88", "s89", "s90", "s91", "s92", "s93", "s94", "s95", "s96", "s97", "vcc", "scc", "memory");
}

extern "C" void kernel_launch(void* const* d_in, const int* in_sizes, int n_in, void* d_out, int out_size,
                              void* d_ws, size_t ws_size, hipStream_t stream) {
  (void)in_sizes; (void)n_in; (void)out_size; (void)d_ws; (void)ws_size;
  hipLaunchKernelGGL(attn_fwd, dim3(NBH), dim3(256), 0, stream, (const unsigned short*)d_in[0], (const unsigned short*)d_in[1],
                     (const unsigned short*)d_in[2], (unsigned short*)d_out);
}
```
